```python
import torch

def _softmax(x, axis=-1):
    return torch.softmax(x, dim=axis)
def _gelu(x):
    return torch.nn.functional.gelu(x, approximate="tanh")
def _silu(x):
    return torch.nn.functional.silu(x)
def _sigmoid(x):
    return torch.sigmoid(x)
def _layernorm(x, axis=-1, eps=1e-5):
    m = x.mean(dim=axis, keepdim=True)
    v = x.var(dim=axis, keepdim=True, unbiased=False)
    return (x - m) / torch.sqrt(v + eps)
def _scatter_add(tgt, idx, src, axis=0):
    return tgt.clone().index_add_(axis, idx, src)
def _segment_sum(src, seg, n):
    out = torch.zeros((n,) + src.shape[1:], dtype=src.dtype)
    return out.index_add_(0, seg, src)
def _pad(x, pw):
    flat = tuple(v for pair in reversed(pw) for v in pair)
    return torch.nn.functional.pad(x, flat)

kernel_name = 'standalone__mha_softmax__near_ridge'

def setup_inputs():

    _w = (lambda a: (lambda t: t.to(torch.bfloat16) if t.is_floating_point() else t)(torch.as_tensor(a)))
    B, H, Sq, Sk, D = 8, 16, 496, 1024, 256
    return {
        "q": _w((lambda *s: torch.randn(*s, dtype=torch.bfloat16) * 0.8)(B, H, Sq, D)),
        "k": _w((lambda *s: torch.randn(*s, dtype=torch.bfloat16) * 0.8)(B, H, Sk, D)),
        "v": _w((lambda *s: torch.randn(*s, dtype=torch.bfloat16) * 0.05)(B, H, Sk, D)),
    }

def reference(q, k, v):
    scale = q.shape[-1] ** -0.5
    scores = torch.einsum("bhqd,bhkd->bhqk", q, k) * scale
    p = _softmax(scores, axis=-1)
    return torch.einsum("bhqk,bhkd->bhqd", p, v)
```

```cpp
#include <hip/hip_runtime.h>
#include <hip/hip_bf16.h>

constexpr int B=8,H=16,SQ=496,SK=1024,D=256;
__device__ __forceinline__ float bf2f(unsigned short x){ return __uint_as_float(((unsigned)x)<<16); }
__global__ __launch_bounds__(256) void naive(const unsigned short* q,const unsigned short* k,const unsigned short* v,__hip_bfloat16* o){
  __shared__ float qs[D]; __shared__ float p[SK]; __shared__ float red[256];
  int row=blockIdx.x; int bh=row/SQ; int t=threadIdx.x;
  qs[t]=bf2f(q[(size_t)row*D+t]); __syncthreads();
  float s[4]; float m=-1e30f;
  for(int j=0;j<4;j++){ const unsigned short* kr=k+((size_t)bh*SK+t+256*j)*D; float a=0;
    for(int d=0;d<D;d+=8){ uint4 w=*(const uint4*)(kr+d); unsigned u[4]={w.x,w.y,w.z,w.w};
      for(int e=0;e<4;e++){ a+=qs[d+2*e]*__uint_as_float(u[e]<<16); a+=qs[d+2*e+1]*__uint_as_float(u[e]&0xffff0000u);} }
    s[j]=a*0.0625f; m=fmaxf(m,s[j]); }
  red[t]=m; __syncthreads();
  for(int st=128;st>0;st>>=1){ if(t<st) red[t]=fmaxf(red[t],red[t+st]); __syncthreads(); }
  m=red[0]; __syncthreads();
  float l=0; for(int j=0;j<4;j++){ float e=__expf(s[j]-m); p[t+256*j]=e; l+=e; }
  red[t]=l; __syncthreads();
  for(int st=128;st>0;st>>=1){ if(t<st) red[t]+=red[t+st]; __syncthreads(); }
  l=red[0];
  float a=0; const unsigned short* vb=v+(size_t)bh*SK*D+t;
  for(int j=0;j<SK;j++) a+=p[j]*bf2f(vb[(size_t)j*D]);
  o[(size_t)row*D+t]=__float2bfloat16(a/l);
}
extern "C" void kernel_launch(void* const* d_in, const int* in_sizes, int n_in, void* d_out, int out_size, void* d_ws, size_t ws_size, hipStream_t stream) {
  naive<<<B*H*SQ,256,0,stream>>>((const unsigned short*)d_in[0],(const unsigned short*)d_in[1],(const unsigned short*)d_in[2],(__hip_bfloat16*)d_out);
}
```

```cpp
#include <hip/hip_runtime.h>
#include <hip/hip_bf16.h>
#define STAMP 0

namespace {
constexpr int SQ = 496, SK = 1024, D = 256;

__device__ __forceinline__ float bflo(unsigned x) { return __uint_as_float(x << 16); }
__device__ __forceinline__ float bfhi(unsigned x) { return __uint_as_float(x & 0xffff0000u); }

__device__ __forceinline__ void slow_rows(const unsigned short* q, const unsigned short* k, const unsigned short* v, unsigned short* o, int row0, float* p) {
  const int lane = __builtin_amdgcn_mbcnt_hi(~0u, __builtin_amdgcn_mbcnt_lo(~0u, 0u));
  for (int r = row0; r < row0 + 64 && r < SQ; ++r) {
    const uint4* qr = (const uint4*)(q + (size_t)r * D);
    float m = -INFINITY;
    bool nan = false;
    for (int j = 0; j < SK / 64; ++j) {
      const uint4* kr = (const uint4*)(k + (size_t)(j * 64 + lane) * D);
      float a = 0.f;
      for (int c = 0; c < D / 8; ++c) {
        const uint4 x = qr[c], y = kr[c];
        a = fmaf(bflo(x.x), bflo(y.x), a); a = fmaf(bfhi(x.x), bfhi(y.x), a);
        a = fmaf(bflo(x.y), bflo(y.y), a); a = fmaf(bfhi(x.y), bfhi(y.y), a);
        a = fmaf(bflo(x.z), bflo(y.z), a); a = fmaf(bfhi(x.z), bfhi(y.z), a);
        a = fmaf(bflo(x.w), bflo(y.w), a); a = fmaf(bfhi(x.w), bfhi(y.w), a);
      }
      a *= 0.0625f;
      p[j * 64 + lane] = a;
      nan |= (a != a);
      m = fmaxf(m, a);
    }
    for (int s = 32; s > 0; s >>= 1) m = fmaxf(m, __shfl_xor(m, s, 64));
    if (__any(nan)) m = NAN;
    float l = 0.f;
    for (int j = 0; j < SK / 64; ++j) { const float e = __expf(p[j * 64 + lane] - m); p[j * 64 + lane] = e; l += e; }
    for (int s = 32; s > 0; s >>= 1) l += __shfl_xor(l, s, 64);
    __builtin_amdgcn_fence(__ATOMIC_RELEASE, "wavefront");
    __builtin_amdgcn_wave_barrier();
    float a0 = 0.f, a1 = 0.f, a2 = 0.f, a3 = 0.f;
    for (int j = 0; j < SK; ++j) {
      const uint2 y = *(const uint2*)(v + (size_t)j * D + lane * 4);
      const float e = p[j];
      a0 = fmaf(e, bflo(y.x), a0); a1 = fmaf(e, bfhi(y.x), a1); a2 = fmaf(e, bflo(y.y), a2); a3 = fmaf(e, bfhi(y.y), a3);
    }
    const float il = 1.f / l;
    __hip_bfloat16* op = (__hip_bfloat16*)(o + (size_t)r * D + lane * 4);
    op[0] = __float2bfloat16(a0 * il); op[1] = __float2bfloat16(a1 * il); op[2] = __float2bfloat16(a2 * il); op[3] = __float2bfloat16(a3 * il);
    __builtin_amdgcn_wave_barrier();
  }
}
}

__global__ __launch_bounds__(256, 1) void mha_fwd_d256(const unsigned short* __restrict__ Q, const unsigned short* __restrict__ K,
                                                       const unsigned short* __restrict__ V, unsigned short* __restrict__ O
) {
  __shared__ __attribute__((aligned(1024))) char lds[163840];
  const unsigned bid = blockIdx.x;
  const unsigned tid = __builtin_amdgcn_readfirstlane(threadIdx.x);
  const unsigned lb = __builtin_amdgcn_readfirstlane((unsigned)(size_t)lds);
  const unsigned long long __attribute__((address_space(4)))* ka =
      (const unsigned long long __attribute__((address_space(4)))*)__builtin_amdgcn_kernarg_segment_ptr();
  unsigned flag;
  asm volatile(
    "s_load_dwordx8 s[24:31], %[ka], 0x0\n"
    "s_lshr_b32 s20, %[tid], 6\n"
    "s_mov_b32 s22, %[lb]\n"
    "v_mbcnt_lo_u32_b32 v124, -1, 0\n"
    "v_mbcnt_hi_u32_b32 v124, -1, v124\n"
    "s_getpc_b64 s[70:71]\n"
    "LPC0%=:\n"
    "s_add_u32 s70, s70, LPC0%=-LPC0%=\n"
    "s_addc_u32 s71, s71, 0\n"
    "s_lshl_b32 s74, s20, 13\n"
    "v_lshl_add_u32 v8, v124, 7, s74\n"
    "v_min_u32 v8, LTAIL%=-LPC0%=, v8\n"
    "global_load_dword v9, v8, s[70:71]\n"
    "s_lshr_b32 s23, %[bid], 4\n"
    "s_lshl_b32 s23, s23, 3\n"
    "s_and_b32 s74, %[bid], 7\n"
    "s_or_b32 s23, s23, s74\n"
    "s_and_b32 s74, %[bid], 8\n"
    "s_lshl_b32 s74, s74, 14\n"
    "s_lshl_b32 s21, s20, 15\n"
    "s_add_u32 s21, s21, s74\n"
    "s_lshl_b32 s74, s23, 19\n"
    "s_mul_i32 s75, s23, 0x3e000\n"
    "s_mov_b32 s42, 0x80a80\n"
    "s_mov_b32 s43, 0x20000\n"
    "s_lshl_b32 s67, s20, 12\n"
    "s_add_u32 s60, s67, s22\n"
    "s_movk_i32 s56, 2688\n"
    "s_movk_i32 s57, 1792\n"
    "s_movk_i32 s58, 896\n"
    "s_movk_i32 s59, 0\n"
    "v_lshrrev_b32 v125, 3, v124\n"
    "v_and_b32 v126, 6, v125\n"
    "v_and_b32 v127, 7, v124\n"
    "v_xor_b32 v126, v126, v127\n"
    "v_lshlrev_b32 v126, 4, v126\n"
    "v_lshl_add_u32 v126, v125, 9, v126\n"
    "v_add_u32 v123, s67, v126\n"
    "v_add_u32 v112, s21, v126\n"
    "s_waitcnt lgkmcnt(0)\n"
    "s_add_u32 s24, s24, s75\n"
    "s_addc_u32 s25, s25, 0\n"
    "s_add_u32 s28, s28, s74\n"
    "s_addc_u32 s29, s29, 0\n"
    "s_add_u32 s30, s30, s75\n"
    "s_addc_u32 s31, s31, 0\n"
    "s_sub_u32 s68, s74, 2688\n"
    "s_subb_u32 s65, 0, 0\n"
    "s_add_u32 s40, s26, s68\n"
    "s_addc_u32 s41, s27, s65\n"
    "s_load_dword s63, s[24:25], s21\n"
    "s_load_dword s64, s[40:41], 0xa80\n"
    "s_load_dword s72, s[28:29], 0x0\n"
    "s_add_u32 m0, s60, 0x0\n"
    "s_nop 0\n"
    "buffer_load_dwordx4 v123, s[40:43], s56 offen offset:0 nt lds\n"
    "buffer_load_dwordx4 v123, s[40:43], s57 offen offset:1024 nt lds\n"
    "buffer_load_dwordx4 v123, s[40:43], s58 offen offset:2048 nt lds\n"
    "buffer_load_dwordx4 v123, s[40:43], s59 offen offset:3072 nt lds\n"
    "v_add_u32 v123, 0x4000, v123\n"
    "s_add_u32 m0, s60, 0x4000\n"
    "s_nop 0\n"
    "buffer_load_dwordx4 v123, s[40:43], s56 offen offset:0 nt lds\n"
    "buffer_load_dwordx4 v123, s[40:43], s57 offen offset:1024 nt lds\n"
    "buffer_load_dwordx4 v123, s[40:43], s58 offen offset:2048 nt lds\n"
    "buffer_load_dwordx4 v123, s[40:43], s59 offen offset:3072 nt lds\n"
    "s_mov_b64 s[48:49], s[24:25]\n"
    "s_mov_b32 s50, 0x3e000\n"
    "s_mov_b32 s51, 0x20000\n"
    "s_lshl_b32 s69, s20, 13\n"
    "s_add_u32 s69, s69, s22\n"
    "s_add_u32 m0, s69, 0x8000\n"
    "s_nop 0\n"
    "buffer_load_dwordx4 v112, s[48:51], 0 offen nt lds\n"
    "v_add_u32 v112, 0x1000, v112\n"
    "s_add_u32 m0, s69, 0x8400\n"
    "s_nop 0\n"
    "buffer_load_dwordx4 v112, s[48:51], 0 offen nt lds\n"
    "v_add_u32 v112, 0x1000, v112\n"
    "s_add_u32 m0, s69, 0x8800\n"
    "s_nop 0\n"
    "buffer_load_dwordx4 v112, s[48:51], 0 offen nt lds\n"
    "v_add_u32 v112, 0x1000, v112\n"
    "s_add_u32 m0, s69, 0x8c00\n"
    "s_nop 0\n"
    "buffer_load_dwordx4 v112, s[48:51], 0 offen nt lds\n"
    "v_add_u32 v112, 0x1000, v112\n"
    "s_waitcnt lgkmcnt(0)\n"
    "s_mov_b64 s[44:45], s[28:29]\n"
    "s_sub_u32 s44, s44, 19072\n"
    "s_subb_u32 s45, s45, 0\n"
    "s_mov_b32 s46, 0x84a80\n"
    "s_mov_b32 s47, 0x20000\n"
    "s_add_u32 s61, s60, 0x10000\n"
    "s_mov_b32 s62, s61\n"
    "s_mov_b32 s63, 0x8000\n"
    "s_mov_b32 s64, 0x8000\n"
    "s_mov_b32 s67, 0x8000\n"
    "s_mov_b32 s68, 0xffff0000\n"
    "s_mov_b32 s65, 6\n"
    "s_mov_b32 s66, 0x3db8aa3b\n"
    "s_mov_b64 s[52:53], s[30:31]\n"
    "s_mov_b32 s54, 0x3e000\n"
    "s_mov_b32 s55, 0x20000\n"
    "v_and_b32 v125, 15, v124\n"
    "v_lshrrev_b32 v126, 4, v124\n"
    "v_and_b32 v4, 6, v125\n"
    "v_xor_b32 v4, v4, v126\n"
    "v_lshlrev_b32 v4, 4, v4\n"
    "v_and_b32 v5, 7, v125\n"
    "v_lshl_add_u32 v4, v5, 7, v4\n"
    "v_lshrrev_b32 v5, 3, v125\n"
    "v_lshl_add_u32 v6, v5, 12, v4\n"
    "v_add_u32 v120, s22, v6\n"
    "v_xor_b32 v121, 64, v120\n"
    "v_lshl_add_u32 v6, v5, 10, v4\n"
    "v_add_u32 v113, s69, v6\n"
    "v_add_u32 v113, 0x8000, v113\n"
    "v_xor_b32 v114, 64, v113\n"
    "v_lshrrev_b32 v4, 2, v124\n"
    "v_and_b32 v4, 7, v4\n"
    "v_lshrrev_b32 v5, 1, v4\n"
    "v_lshlrev_b32 v5, 5, v5\n"
    "v_lshl_add_u32 v5, v4, 7, v5\n"
    "v_and_b32 v4, 3, v124\n"
    "v_lshl_add_u32 v5, v4, 3, v5\n"
    "v_lshrrev_b32 v4, 5, v124\n"
    "v_lshl_add_u32 v5, v4, 12, v5\n"
    "v_add_u32 v5, s22, v5\n"
    "v_add_u32 v122, 0x10000, v5\n"
    "v_mov_b32 v116, 0\n"
    "v_mov_b32 v117, 0\n"
    "v_mov_b32 v118, 0\n"
    "v_mov_b32 v119, 0\n"
    "s_nop 0\n"
    "s_nop 0\n"
    "s_nop 0\n"
    "s_nop 0\n"
    "s_nop 0\n"
    "s_waitcnt vmcnt(8)\n"
    "s_add_u32 m0, s69, 0x9000\n"
    "s_nop 0\n"
    "buffer_load_dwordx4 v112, s[48:51], 0 offen nt lds\n"
    "v_add_u32 v112, 0x1000, v112\n"
    "s_add_u32 m0, s69, 0x9400\n"
    "s_nop 0\n"
    "buffer_load_dwordx4 v112, s[48:51], 0 offen nt lds\n"
    "v_add_u32 v112, 0x1000, v112\n"
    "s_add_u32 m0, s69, 0x9800\n"
    "s_nop 0\n"
    "buffer_load_dwordx4 v112, s[48:51], 0 offen nt lds\n"
    "v_add_u32 v112, 0x1000, v112\n"
    "s_add_u32 m0, s69, 0x9c00\n"
    "s_nop 0\n"
    "buffer_load_dwordx4 v112, s[48:51], 0 offen nt lds\n"
    "v_add_u32 v112, 0xffff9080, v112\n"
    "s_waitcnt vmcnt(8)\n"
    "s_barrier\n"
    "ds_read_b128 v[80:83], v120 offset:0\n"
    "ds_read_b128 v[84:87], v121 offset:0\n"
    "ds_read_b128 v[88:91], v120 offset:8192\n"
    "ds_read_b128 v[92:95], v121 offset:8192\n"
    "ds_read_b128 v[96:99], v120 offset:16384\n"
    "ds_read_b128 v[100:103], v121 offset:16384\n"
    "ds_read_b128 v[104:107], v120 offset:24576\n"
    "ds_read_b128 v[108:111], v121 offset:24576\n"
    "s_waitcnt vmcnt(6)\n"
    "ds_read_b128 v[128:131], v113 offset:0\n"
    "ds_read_b128 v[132:135], v114 offset:0\n"
    "s_add_u32 m0, s69, 0x10000\n"
    "s_nop 0\n"
    "buffer_load_dwordx4 v112, s[48:51], 0 offen nt lds\n"
    "v_add_u32 v112, 0x1000, v112\n"
    "s_add_u32 m0, s69, 0x10400\n"
    "s_nop 0\n"
    "buffer_load_dwordx4 v112, s[48:51], 0 offen nt lds\n"
    "v_add_u32 v112, 0x1000, v112\n"
    "s_waitcnt lgkmcnt(0)\n"
    "v_lshlrev_b32 v32, 16, v128\n"
    "v_and_b32 v33, 0xffff0000, v128\n"
    "v_lshlrev_b32 v34, 16, v129\n"
    "v_and_b32 v35, 0xffff0000, v129\n"
    "v_lshlrev_b32 v36, 16, v130\n"
    "v_and_b32 v37, 0xffff0000, v130\n"
    "v_lshlrev_b32 v38, 16, v131\n"
    "v_and_b32 v39, 0xffff0000, v131\n"
    "v_lshlrev_b32 v40, 16, v132\n"
    "v_and_b32 v41, 0xffff0000, v132\n"
    "v_lshlrev_b32 v42, 16, v133\n"
    "v_and_b32 v43, 0xffff0000, v133\n"
    "v_lshlrev_b32 v44, 16, v134\n"
    "v_and_b32 v45, 0xffff0000, v134\n"
    "v_lshlrev_b32 v46, 16, v135\n"
    "v_and_b32 v47, 0xffff0000, v135\n"
    "v_mul_f32 v32, s66, v32\n"
    "v_mul_f32 v33, s66, v33\n"
    "v_mul_f32 v34, s66, v34\n"
    "v_mul_f32 v35, s66, v35\n"
    "v_mul_f32 v36, s66, v36\n"
    "v_mul_f32 v37, s66, v37\n"
    "v_mul_f32 v38, s66, v38\n"
    "v_mul_f32 v39, s66, v39\n"
    "v_mul_f32 v40, s66, v40\n"
    "v_mul_f32 v41, s66, v41\n"
    "v_mul_f32 v42, s66, v42\n"
    "v_mul_f32 v43, s66, v43\n"
    "v_mul_f32 v44, s66, v44\n"
    "v_mul_f32 v45, s66, v45\n"
    "v_mul_f32 v46, s66, v46\n"
    "v_mul_f32 v47, s66, v47\n"
    "v_cvt_pk_bf16_f32 v128, v32, v33\n"
    "v_cvt_pk_bf16_f32 v129, v34, v35\n"
    "v_cvt_pk_bf16_f32 v130, v36, v37\n"
    "v_cvt_pk_bf16_f32 v131, v38, v39\n"
    "v_cvt_pk_bf16_f32 v132, v40, v41\n"
    "v_cvt_pk_bf16_f32 v133, v42, v43\n"
    "v_cvt_pk_bf16_f32 v134, v44, v45\n"
    "v_cvt_pk_bf16_f32 v135, v46, v47\n"
    "s_waitcnt vmcnt(6)\n"
    "ds_read_b128 v[160:163], v113 offset:2048\n"
    "ds_read_b128 v[164:167], v114 offset:2048\n"
    "s_add_u32 m0, s69, 0x10800\n"
    "s_nop 0\n"
    "buffer_load_dwordx4 v112, s[48:51], 0 offen nt lds\n"
    "v_add_u32 v112, 0x1000, v112\n"
    "s_add_u32 m0, s69, 0x10c00\n"
    "s_nop 0\n"
    "buffer_load_dwordx4 v112, s[48:51], 0 offen nt lds\n"
    "v_add_u32 v112, 0x1000, v112\n"
    "s_waitcnt lgkmcnt(2)\n"
    "v_mfma_f32_16x16x32_bf16 v[0:3], v[80:83], v[128:131], 0\n"
    "v_mfma_f32_16x16x32_bf16 v[0:3], v[84:87], v[132:135], v[0:3]\n"
    "v_mfma_f32_16x16x32_bf16 v[48:51], v[88:91], v[128:131], 0\n"
    "v_mfma_f32_16x16x32_bf16 v[48:51], v[92:95], v[132:135], v[48:51]\n"
    "v_mfma_f32_16x16x32_bf16 v[16:19], v[96:99], v[128:131], 0\n"
    "v_mfma_f32_16x16x32_bf16 v[16:19], v[100:103], v[132:135], v[16:19]\n"
    "v_mfma_f32_16x16x32_bf16 v[64:67], v[104:107], v[128:131], 0\n"
    "v_mfma_f32_16x16x32_bf16 v[64:67], v[108:111], v[132:135], v[64:67]\n"
    "s_waitcnt lgkmcnt(0)\n"
    "v_lshlrev_b32 v32, 16, v160\n"
    "v_and_b32 v33, 0xffff0000, v160\n"
    "v_lshlrev_b32 v34, 16, v161\n"
    "v_and_b32 v35, 0xffff0000, v161\n"
    "v_lshlrev_b32 v36, 16, v162\n"
    "v_and_b32 v37, 0xffff0000, v162\n"
    "v_lshlrev_b32 v38, 16, v163\n"
    "v_and_b32 v39, 0xffff0000, v163\n"
    "v_lshlrev_b32 v40, 16, v164\n"
    "v_and_b32 v41, 0xffff0000, v164\n"
    "v_lshlrev_b32 v42, 16, v165\n"
    "v_and_b32 v43, 0xffff0000, v165\n"
    "v_lshlrev_b32 v44, 16, v166\n"
    "v_and_b32 v45, 0xffff0000, v166\n"
    "v_lshlrev_b32 v46, 16, v167\n"
    "v_and_b32 v47, 0xffff0000, v167\n"
    "v_mul_f32 v32, s66, v32\n"
    "v_mul_f32 v33, s66, v33\n"
    "v_mul_f32 v34, s66, v34\n"
    "v_mul_f32 v35, s66, v35\n"
    "v_mul_f32 v36, s66, v36\n"
    "v_mul_f32 v37, s66, v37\n"
    "v_mul_f32 v38, s66, v38\n"
    "v_mul_f32 v39, s66, v39\n"
    "v_mul_f32 v40, s66, v40\n"
    "v_mul_f32 v41, s66, v41\n"
    "v_mul_f32 v42, s66, v42\n"
    "v_mul_f32 v43, s66, v43\n"
    "v_mul_f32 v44, s66, v44\n"
    "v_mul_f32 v45, s66, v45\n"
    "v_mul_f32 v46, s66, v46\n"
    "v_mul_f32 v47, s66, v47\n"
    "v_cvt_pk_bf16_f32 v160, v32, v33\n"
    "v_cvt_pk_bf16_f32 v161, v34, v35\n"
    "v_cvt_pk_bf16_f32 v162, v36, v37\n"
    "v_cvt_pk_bf16_f32 v163, v38, v39\n"
    "v_cvt_pk_bf16_f32 v164, v40, v41\n"
    "v_cvt_pk_bf16_f32 v165, v42, v43\n"
    "v_cvt_pk_bf16_f32 v166, v44, v45\n"
    "v_cvt_pk_bf16_f32 v167, v46, v47\n"
    "s_waitcnt vmcnt(6)\n"
    "ds_read_b128 v[192:195], v113 offset:4096\n"
    "ds_read_b128 v[196:199], v114 offset:4096\n"
    "s_add_u32 m0, s69, 0x11000\n"
    "s_nop 0\n"
    "buffer_load_dwordx4 v112, s[48:51], 0 offen nt lds\n"
    "v_add_u32 v112, 0x1000, v112\n"
    "s_add_u32 m0, s69, 0x11400\n"
    "s_nop 0\n"
    "buffer_load_dwordx4 v112, s[48:51], 0 offen nt lds\n"
    "v_add_u32 v112, 0x1000, v112\n"
    "v_mfma_f32_16x16x32_bf16 v[4:7], v[80:83], v[160:163], 0\n"
    "v_mfma_f32_16x16x32_bf16 v[4:7], v[84:87], v[164:167], v[4:7]\n"
    "v_mfma_f32_16x16x32_bf16 v[52:55], v[88:91], v[160:163], 0\n"
    "v_mfma_f32_16x16x32_bf16 v[52:55], v[92:95], v[164:167], v[52:55]\n"
    "v_mfma_f32_16x16x32_bf16 v[20:23], v[96:99], v[160:163], 0\n"
    "v_mfma_f32_16x16x32_bf16 v[20:23], v[100:103], v[164:167], v[20:23]\n"
    "v_mfma_f32_16x16x32_bf16 v[68:71], v[104:107], v[160:163], 0\n"
    "v_mfma_f32_16x16x32_bf16 v[68:71], v[108:111], v[164:167], v[68:71]\n"
    "s_waitcnt lgkmcnt(0)\n"
    "v_lshlrev_b32 v32, 16, v192\n"
    "v_and_b32 v33, 0xffff0000, v192\n"
    "v_lshlrev_b32 v34, 16, v193\n"
    "v_and_b32 v35, 0xffff0000, v193\n"
    "v_lshlrev_b32 v36, 16, v194\n"
    "v_and_b32 v37, 0xffff0000, v194\n"
    "v_lshlrev_b32 v38, 16, v195\n"
    "v_and_b32 v39, 0xffff0000, v195\n"
    "v_lshlrev_b32 v40, 16, v196\n"
    "v_and_b32 v41, 0xffff0000, v196\n"
    "v_lshlrev_b32 v42, 16, v197\n"
    "v_and_b32 v43, 0xffff0000, v197\n"
    "v_lshlrev_b32 v44, 16, v198\n"
    "v_and_b32 v45, 0xffff0000, v198\n"
    "v_lshlrev_b32 v46, 16, v199\n"
    "v_and_b32 v47, 0xffff0000, v199\n"
    "v_mul_f32 v32, s66, v32\n"
    "v_mul_f32 v33, s66, v33\n"
    "v_mul_f32 v34, s66, v34\n"
    "v_mul_f32 v35, s66, v35\n"
    "v_mul_f32 v36, s66, v36\n"
    "v_mul_f32 v37, s66, v37\n"
    "v_mul_f32 v38, s66, v38\n"
    "v_mul_f32 v39, s66, v39\n"
    "v_mul_f32 v40, s66, v40\n"
    "v_mul_f32 v41, s66, v41\n"
    "v_mul_f32 v42, s66, v42\n"
    "v_mul_f32 v43, s66, v43\n"
    "v_mul_f32 v44, s66, v44\n"
    "v_mul_f32 v45, s66, v45\n"
    "v_mul_f32 v46, s66, v46\n"
    "v_mul_f32 v47, s66, v47\n"
    "v_cvt_pk_bf16_f32 v192, v32, v33\n"
    "v_cvt_pk_bf16_f32 v193, v34, v35\n"
    "v_cvt_pk_bf16_f32 v194, v36, v37\n"
    "v_cvt_pk_bf16_f32 v195, v38, v39\n"
    "v_cvt_pk_bf16_f32 v196, v40, v41\n"
    "v_cvt_pk_bf16_f32 v197, v42, v43\n"
    "v_cvt_pk_bf16_f32 v198, v44, v45\n"
    "v_cvt_pk_bf16_f32 v199, v46, v47\n"
    "s_waitcnt vmcnt(6)\n"
    "ds_read_b128 v[224:227], v113 offset:6144\n"
    "ds_read_b128 v[228:231], v114 offset:6144\n"
    "v_add_u32 v113, 0x8000, v113\n"
    "v_add_u32 v114, 0x8000, v114\n"
    "s_add_u32 m0, s69, 0x11800\n"
    "s_nop 0\n"
    "buffer_load_dwordx4 v112, s[48:51], 0 offen nt lds\n"
    "v_add_u32 v112, 0x1000, v112\n"
    "s_add_u32 m0, s69, 0x11c00\n"
    "s_nop 0\n"
    "buffer_load_dwordx4 v112, s[48:51], 0 offen nt lds\n"
    "v_add_u32 v112, 0xffff9080, v112\n"
    "v_mfma_f32_16x16x32_bf16 v[8:11], v[80:83], v[192:195], 0\n"
    "v_mfma_f32_16x16x32_bf16 v[8:11], v[84:87], v[196:199], v[8:11]\n"
    "v_mfma_f32_16x16x32_bf16 v[56:59], v[88:91], v[192:195], 0\n"
    "v_mfma_f32_16x16x32_bf16 v[56:59], v[92:95], v[196:199], v[56:59]\n"
    "v_mfma_f32_16x16x32_bf16 v[24:27], v[96:99], v[192:195], 0\n"
    "v_mfma_f32_16x16x32_bf16 v[24:27], v[100:103], v[196:199], v[24:27]\n"
    "v_mfma_f32_16x16x32_bf16 v[72:75], v[104:107], v[192:195], 0\n"
    "v_mfma_f32_16x16x32_bf16 v[72:75], v[108:111], v[196:199], v[72:75]\n"
    "s_waitcnt lgkmcnt(0)\n"
    "v_lshlrev_b32 v32, 16, v224\n"
    "v_and_b32 v33, 0xffff0000, v224\n"
    "v_lshlrev_b32 v34, 16, v225\n"
    "v_and_b32 v35, 0xffff0000, v225\n"
    "v_lshlrev_b32 v36, 16, v226\n"
    "v_and_b32 v37, 0xffff0000, v226\n"
    "v_lshlrev_b32 v38, 16, v227\n"
    "v_and_b32 v39, 0xffff0000, v227\n"
    "v_lshlrev_b32 v40, 16, v228\n"
    "v_and_b32 v41, 0xffff0000, v228\n"
    "v_lshlrev_b32 v42, 16, v229\n"
    "v_and_b32 v43, 0xffff0000, v229\n"
    "v_lshlrev_b32 v44, 16, v230\n"
    "v_and_b32 v45, 0xffff0000, v230\n"
    "v_lshlrev_b32 v46, 16, v231\n"
    "v_and_b32 v47, 0xffff0000, v231\n"
    "v_mul_f32 v32, s66, v32\n"
    "v_mul_f32 v33, s66, v33\n"
    "v_mul_f32 v34, s66, v34\n"
    "v_mul_f32 v35, s66, v35\n"
    "v_mul_f32 v36, s66, v36\n"
    "v_mul_f32 v37, s66, v37\n"
    "v_mul_f32 v38, s66, v38\n"
    "v_mul_f32 v39, s66, v39\n"
    "v_mul_f32 v40, s66, v40\n"
    "v_mul_f32 v41, s66, v41\n"
    "v_mul_f32 v42, s66, v42\n"
    "v_mul_f32 v43, s66, v43\n"
    "v_mul_f32 v44, s66, v44\n"
    "v_mul_f32 v45, s66, v45\n"
    "v_mul_f32 v46, s66, v46\n"
    "v_mul_f32 v47, s66, v47\n"
    "v_cvt_pk_bf16_f32 v224, v32, v33\n"
    "v_cvt_pk_bf16_f32 v225, v34, v35\n"
    "v_cvt_pk_bf16_f32 v226, v36, v37\n"
    "v_cvt_pk_bf16_f32 v227, v38, v39\n"
    "v_cvt_pk_bf16_f32 v228, v40, v41\n"
    "v_cvt_pk_bf16_f32 v229, v42, v43\n"
    "v_cvt_pk_bf16_f32 v230, v44, v45\n"
    "v_cvt_pk_bf16_f32 v231, v46, v47\n"
    "s_waitcnt vmcnt(6)\n"
    "ds_read_b128 v[136:139], v113 offset:0\n"
    "ds_read_b128 v[140:143], v114 offset:0\n"
    "s_add_u32 m0, s69, 0x18000\n"
    "s_nop 0\n"
    "buffer_load_dwordx4 v112, s[48:51], 0 offen nt lds\n"
    "v_add_u32 v112, 0x1000, v112\n"
    "s_add_u32 m0, s69, 0x18400\n"
    "s_nop 0\n"
    "buffer_load_dwordx4 v112, s[48:51], 0 offen nt lds\n"
    "v_add_u32 v112, 0x1000, v112\n"
    "v_mfma_f32_16x16x32_bf16 v[12:15], v[80:83], v[224:227], 0\n"
    "v_mfma_f32_16x16x32_bf16 v[12:15], v[84:87], v[228:231], v[12:15]\n"
    "v_mfma_f32_16x16x32_bf16 v[60:63], v[88:91], v[224:227], 0\n"
    "v_mfma_f32_16x16x32_bf16 v[60:63], v[92:95], v[228:231], v[60:63]\n"
    "v_mfma_f32_16x16x32_bf16 v[28:31], v[96:99], v[224:227], 0\n"
    "v_mfma_f32_16x16x32_bf16 v[28:31], v[100:103], v[228:231], v[28:31]\n"
    "v_mfma_f32_16x16x32_bf16 v[76:79], v[104:107], v[224:227], 0\n"
    "v_mfma_f32_16x16x32_bf16 v[76:79], v[108:111], v[228:231], v[76:79]\n"
    "ds_read_b128 v[80:83], v120 offset:1024\n"
    "ds_read_b128 v[84:87], v121 offset:1024\n"
    "ds_read_b128 v[88:91], v120 offset:9216\n"
    "ds_read_b128 v[92:95], v121 offset:9216\n"
    "ds_read_b128 v[96:99], v120 offset:17408\n"
    "ds_read_b128 v[100:103], v121 offset:17408\n"
    "ds_read_b128 v[104:107], v120 offset:25600\n"
    "ds_read_b128 v[108:111], v121 offset:25600\n"
    "s_waitcnt lgkmcnt(8)\n"
    "v_lshlrev_b32 v32, 16, v136\n"
    "v_and_b32 v33, 0xffff0000, v136\n"
    "v_lshlrev_b32 v34, 16, v137\n"
    "v_and_b32 v35, 0xffff0000, v137\n"
    "v_lshlrev_b32 v36, 16, v138\n"
    "v_and_b32 v37, 0xffff0000, v138\n"
    "v_lshlrev_b32 v38, 16, v139\n"
    "v_and_b32 v39, 0xffff0000, v139\n"
    "v_lshlrev_b32 v40, 16, v140\n"
    "v_and_b32 v41, 0xffff0000, v140\n"
    "v_lshlrev_b32 v42, 16, v141\n"
    "v_and_b32 v43, 0xffff0000, v141\n"
    "v_lshlrev_b32 v44, 16, v142\n"
    "v_and_b32 v45, 0xffff0000, v142\n"
    "v_lshlrev_b32 v46, 16, v143\n"
    "v_and_b32 v47, 0xffff0000, v143\n"
    "v_mul_f32 v32, s66, v32\n"
    "v_mul_f32 v33, s66, v33\n"
    "v_mul_f32 v34, s66, v34\n"
    "v_mul_f32 v35, s66, v35\n"
    "v_mul_f32 v36, s66, v36\n"
    "v_mul_f32 v37, s66, v37\n"
    "v_mul_f32 v38, s66, v38\n"
    "v_mul_f32 v39, s66, v39\n"
    "v_mul_f32 v40, s66, v40\n"
    "v_mul_f32 v41, s66, v41\n"
    "v_mul_f32 v42, s66, v42\n"
    "v_mul_f32 v43, s66, v43\n"
    "v_mul_f32 v44, s66, v44\n"
    "v_mul_f32 v45, s66, v45\n"
    "v_mul_f32 v46, s66, v46\n"
    "v_mul_f32 v47, s66, v47\n"
    "v_cvt_pk_bf16_f32 v136, v32, v33\n"
    "v_cvt_pk_bf16_f32 v137, v34, v35\n"
    "v_cvt_pk_bf16_f32 v138, v36, v37\n"
    "v_cvt_pk_bf16_f32 v139, v38, v39\n"
    "v_cvt_pk_bf16_f32 v140, v40, v41\n"
    "v_cvt_pk_bf16_f32 v141, v42, v43\n"
    "v_cvt_pk_bf16_f32 v142, v44, v45\n"
    "v_cvt_pk_bf16_f32 v143, v46, v47\n"
    "s_waitcnt lgkmcnt(0)\n"
    "s_waitcnt vmcnt(6)\n"
    "ds_read_b128 v[168:171], v113 offset:2048\n"
    "ds_read_b128 v[172:175], v114 offset:2048\n"
    "s_add_u32 m0, s69, 0x18800\n"
    "s_nop 0\n"
    "buffer_load_dwordx4 v112, s[48:51], 0 offen nt lds\n"
    "v_add_u32 v112, 0x1000, v112\n"
    "s_add_u32 m0, s69, 0x18c00\n"
    "s_nop 0\n"
    "buffer_load_dwordx4 v112, s[48:51], 0 offen nt lds\n"
    "v_add_u32 v112, 0x1000, v112\n"
    "v_mfma_f32_16x16x32_bf16 v[0:3], v[80:83], v[136:139], v[0:3]\n"
    "v_mfma_f32_16x16x32_bf16 v[0:3], v[84:87], v[140:143], v[0:3]\n"
    "v_mfma_f32_16x16x32_bf16 v[48:51], v[88:91], v[136:139], v[48:51]\n"
    "v_mfma_f32_16x16x32_bf16 v[48:51], v[92:95], v[140:143], v[48:51]\n"
    "v_mfma_f32_16x16x32_bf16 v[16:19], v[96:99], v[136:139], v[16:19]\n"
    "v_mfma_f32_16x16x32_bf16 v[16:19], v[100:103], v[140:143], v[16:19]\n"
    "v_mfma_f32_16x16x32_bf16 v[64:67], v[104:107], v[136:139], v[64:67]\n"
    "v_mfma_f32_16x16x32_bf16 v[64:67], v[108:111], v[140:143], v[64:67]\n"
    "s_waitcnt lgkmcnt(0)\n"
    "v_lshlrev_b32 v32, 16, v168\n"
    "v_and_b32 v33, 0xffff0000, v168\n"
    "v_lshlrev_b32 v34, 16, v169\n"
    "v_and_b32 v35, 0xffff0000, v169\n"
    "v_lshlrev_b32 v36, 16, v170\n"
    "v_and_b32 v37, 0xffff0000, v170\n"
    "v_lshlrev_b32 v38, 16, v171\n"
    "v_and_b32 v39, 0xffff0000, v171\n"
    "v_lshlrev_b32 v40, 16, v172\n"
    "v_and_b32 v41, 0xffff0000, v172\n"
    "v_lshlrev_b32 v42, 16, v173\n"
    "v_and_b32 v43, 0xffff0000, v173\n"
    "v_lshlrev_b32 v44, 16, v174\n"
    "v_and_b32 v45, 0xffff0000, v174\n"
    "v_lshlrev_b32 v46, 16, v175\n"
    "v_and_b32 v47, 0xffff0000, v175\n"
    "v_mul_f32 v32, s66, v32\n"
    "v_mul_f32 v33, s66, v33\n"
    "v_mul_f32 v34, s66, v34\n"
    "v_mul_f32 v35, s66, v35\n"
    "v_mul_f32 v36, s66, v36\n"
    "v_mul_f32 v37, s66, v37\n"
    "v_mul_f32 v38, s66, v38\n"
    "v_mul_f32 v39, s66, v39\n"
    "v_mul_f32 v40, s66, v40\n"
    "v_mul_f32 v41, s66, v41\n"
    "v_mul_f32 v42, s66, v42\n"
    "v_mul_f32 v43, s66, v43\n"
    "v_mul_f32 v44, s66, v44\n"
    "v_mul_f32 v45, s66, v45\n"
    "v_mul_f32 v46, s66, v46\n"
    "v_mul_f32 v47, s66, v47\n"
    "v_cvt_pk_bf16_f32 v168, v32, v33\n"
    "v_cvt_pk_bf16_f32 v169, v34, v35\n"
    "v_cvt_pk_bf16_f32 v170, v36, v37\n"
    "v_cvt_pk_bf16_f32 v171, v38, v39\n"
    "v_cvt_pk_bf16_f32 v172, v40, v41\n"
    "v_cvt_pk_bf16_f32 v173, v42, v43\n"
    "v_cvt_pk_bf16_f32 v174, v44, v45\n"
    "v_cvt_pk_bf16_f32 v175, v46, v47\n"
    "s_waitcnt vmcnt(6)\n"
    "ds_read_b128 v[200:203], v113 offset:4096\n"
    "ds_read_b128 v[204:207], v114 offset:4096\n"
    "s_add_u32 m0, s69, 0x19000\n"
    "s_nop 0\n"
    "buffer_load_dwordx4 v112, s[48:51], 0 offen nt lds\n"
    "v_add_u32 v112, 0x1000, v112\n"
    "s_add_u32 m0, s69, 0x19400\n"
    "s_nop 0\n"
    "buffer_load_dwordx4 v112, s[48:51], 0 offen nt lds\n"
    "v_add_u32 v112, 0x1000, v112\n"
    "v_mfma_f32_16x16x32_bf16 v[4:7], v[80:83], v[168:171], v[4:7]\n"
    "v_mfma_f32_16x16x32_bf16 v[4:7], v[84:87], v[172:175], v[4:7]\n"
    "v_mfma_f32_16x16x32_bf16 v[52:55], v[88:91], v[168:171], v[52:55]\n"
    "v_mfma_f32_16x16x32_bf16 v[52:55], v[92:95], v[172:175], v[52:55]\n"
    "v_mfma_f32_16x16x32_bf16 v[20:23], v[96:99], v[168:171], v[20:23]\n"
    "v_mfma_f32_16x16x32_bf16 v[20:23], v[100:103], v[172:175], v[20:23]\n"
    "v_mfma_f32_16x16x32_bf16 v[68:71], v[104:107], v[168:171], v[68:71]\n"
    "v_mfma_f32_16x16x32_bf16 v[68:71], v[108:111], v[172:175], v[68:71]\n"
    "s_waitcnt lgkmcnt(0)\n"
    "v_lshlrev_b32 v32, 16, v200\n"
    "v_and_b32 v33, 0xffff0000, v200\n"
    "v_lshlrev_b32 v34, 16, v201\n"
    "v_and_b32 v35, 0xffff0000, v201\n"
    "v_lshlrev_b32 v36, 16, v202\n"
    "v_and_b32 v37, 0xffff0000, v202\n"
    "v_lshlrev_b32 v38, 16, v203\n"
    "v_and_b32 v39, 0xffff0000, v203\n"
    "v_lshlrev_b32 v40, 16, v204\n"
    "v_and_b32 v41, 0xffff0000, v204\n"
    "v_lshlrev_b32 v42, 16, v205\n"
    "v_and_b32 v43, 0xffff0000, v205\n"
    "v_lshlrev_b32 v44, 16, v206\n"
    "v_and_b32 v45, 0xffff0000, v206\n"
    "v_lshlrev_b32 v46, 16, v207\n"
    "v_and_b32 v47, 0xffff0000, v207\n"
    "v_mul_f32 v32, s66, v32\n"
    "v_mul_f32 v33, s66, v33\n"
    "v_mul_f32 v34, s66, v34\n"
    "v_mul_f32 v35, s66, v35\n"
    "v_mul_f32 v36, s66, v36\n"
    "v_mul_f32 v37, s66, v37\n"
    "v_mul_f32 v38, s66, v38\n"
    "v_mul_f32 v39, s66, v39\n"
    "v_mul_f32 v40, s66, v40\n"
    "v_mul_f32 v41, s66, v41\n"
    "v_mul_f32 v42, s66, v42\n"
    "v_mul_f32 v43, s66, v43\n"
    "v_mul_f32 v44, s66, v44\n"
    "v_mul_f32 v45, s66, v45\n"
    "v_mul_f32 v46, s66, v46\n"
    "v_mul_f32 v47, s66, v47\n"
    "v_cvt_pk_bf16_f32 v200, v32, v33\n"
    "v_cvt_pk_bf16_f32 v201, v34, v35\n"
    "v_cvt_pk_bf16_f32 v202, v36, v37\n"
    "v_cvt_pk_bf16_f32 v203, v38, v39\n"
    "v_cvt_pk_bf16_f32 v204, v40, v41\n"
    "v_cvt_pk_bf16_f32 v205, v42, v43\n"
    "v_cvt_pk_bf16_f32 v206, v44, v45\n"
    "v_cvt_pk_bf16_f32 v207, v46, v47\n"
    "s_waitcnt vmcnt(6)\n"
    "ds_read_b128 v[232:235], v113 offset:6144\n"
    "ds_read_b128 v[236:239], v114 offset:6144\n"
    "v_add_u32 v113, 0x8000, v113\n"
    "v_add_u32 v114, 0x8000, v114\n"
    "s_add_u32 m0, s69, 0x19800\n"
    "s_nop 0\n"
    "buffer_load_dwordx4 v112, s[48:51], 0 offen nt lds\n"
    "v_add_u32 v112, 0x1000, v112\n"
    "s_add_u32 m0, s69, 0x19c00\n"
    "s_nop 0\n"
    "buffer_load_dwordx4 v112, s[48:51], 0 offen nt lds\n"
    "v_add_u32 v112, 0xffff9080, v112\n"
    "v_mfma_f32_16x16x32_bf16 v[8:11], v[80:83], v[200:203], v[8:11]\n"
    "v_mfma_f32_16x16x32_bf16 v[8:11], v[84:87], v[204:207], v[8:11]\n"
    "v_mfma_f32_16x16x32_bf16 v[56:59], v[88:91], v[200:203], v[56:59]\n"
    "v_mfma_f32_16x16x32_bf16 v[56:59], v[92:95], v[204:207], v[56:59]\n"
    "v_mfma_f32_16x16x32_bf16 v[24:27], v[96:99], v[200:203], v[24:27]\n"
    "v_mfma_f32_16x16x32_bf16 v[24:27], v[100:103], v[204:207], v[24:27]\n"
    "v_mfma_f32_16x16x32_bf16 v[72:75], v[104:107], v[200:203], v[72:75]\n"
    "v_mfma_f32_16x16x32_bf16 v[72:75], v[108:111], v[204:207], v[72:75]\n"
    "s_waitcnt lgkmcnt(0)\n"
    "s_barrier\n"
    "v_lshlrev_b32 v32, 16, v232\n"
    "v_and_b32 v33, 0xffff0000, v232\n"
    "v_lshlrev_b32 v34, 16, v233\n"
    "v_and_b32 v35, 0xffff0000, v233\n"
    "v_lshlrev_b32 v36, 16, v234\n"
    "v_and_b32 v37, 0xffff0000, v234\n"
    "v_lshlrev_b32 v38, 16, v235\n"
    "v_and_b32 v39, 0xffff0000, v235\n"
    "v_lshlrev_b32 v40, 16, v236\n"
    "v_and_b32 v41, 0xffff0000, v236\n"
    "v_lshlrev_b32 v42, 16, v237\n"
    "v_and_b32 v43, 0xffff0000, v237\n"
    "v_lshlrev_b32 v44, 16, v238\n"
    "v_and_b32 v45, 0xffff0000, v238\n"
    "v_lshlrev_b32 v46, 16, v239\n"
    "v_and_b32 v47, 0xffff0000, v239\n"
    "v_mul_f32 v32, s66, v32\n"
    "v_mul_f32 v33, s66, v33\n"
    "v_mul_f32 v34, s66, v34\n"
    "v_mul_f32 v35, s66, v35\n"
    "v_mul_f32 v36, s66, v36\n"
    "v_mul_f32 v37, s66, v37\n"
    "v_mul_f32 v38, s66, v38\n"
    "v_mul_f32 v39, s66, v39\n"
    "v_mul_f32 v40, s66, v40\n"
    "v_mul_f32 v41, s66, v41\n"
    "v_mul_f32 v42, s66, v42\n"
    "v_mul_f32 v43, s66, v43\n"
    "v_mul_f32 v44, s66, v44\n"
    "v_mul_f32 v45, s66, v45\n"
    "v_mul_f32 v46, s66, v46\n"
    "v_mul_f32 v47, s66, v47\n"
    "v_cvt_pk_bf16_f32 v232, v32, v33\n"
    "v_cvt_pk_bf16_f32 v233, v34, v35\n"
    "v_cvt_pk_bf16_f32 v234, v36, v37\n"
    "v_cvt_pk_bf16_f32 v235, v38, v39\n"
    "v_cvt_pk_bf16_f32 v236, v40, v41\n"
    "v_cvt_pk_bf16_f32 v237, v42, v43\n"
    "v_cvt_pk_bf16_f32 v238, v44, v45\n"
    "v_cvt_pk_bf16_f32 v239, v46, v47\n"
    "s_waitcnt vmcnt(6)\n"
    "ds_read_b128 v[144:147], v113 offset:0\n"
    "ds_read_b128 v[148:151], v114 offset:0\n"
    "s_add_u32 m0, s69, 0x20000\n"
    "s_nop 0\n"
    "buffer_load_dwordx4 v112, s[48:51], 0 offen nt lds\n"
    "v_add_u32 v112, 0x1000, v112\n"
    "s_add_u32 m0, s69, 0x20400\n"
    "s_nop 0\n"
    "buffer_load_dwordx4 v112, s[48:51], 0 offen nt lds\n"
    "v_add_u32 v112, 0x1000, v112\n"
    "v_mfma_f32_16x16x32_bf16 v[12:15], v[80:83], v[232:235], v[12:15]\n"
    "v_mfma_f32_16x16x32_bf16 v[12:15], v[84:87], v[236:239], v[12:15]\n"
    "v_mfma_f32_16x16x32_bf16 v[60:63], v[88:91], v[232:235], v[60:63]\n"
    "v_mfma_f32_16x16x32_bf16 v[60:63], v[92:95], v[236:239], v[60:63]\n"
    "v_mfma_f32_16x16x32_bf16 v[28:31], v[96:99], v[232:235], v[28:31]\n"
    "v_mfma_f32_16x16x32_bf16 v[28:31], v[100:103], v[236:239], v[28:31]\n"
    "v_mfma_f32_16x16x32_bf16 v[76:79], v[104:107], v[232:235], v[76:79]\n"
    "v_mfma_f32_16x16x32_bf16 v[76:79], v[108:111], v[236:239], v[76:79]\n"
    "ds_read_b128 v[80:83], v120 offset:2048\n"
    "ds_read_b128 v[84:87], v121 offset:2048\n"
    "ds_read_b128 v[88:91], v120 offset:10240\n"
    "ds_read_b128 v[92:95], v121 offset:10240\n"
    "ds_read_b128 v[96:99], v120 offset:18432\n"
    "ds_read_b128 v[100:103], v121 offset:18432\n"
    "ds_read_b128 v[104:107], v120 offset:26624\n"
    "ds_read_b128 v[108:111], v121 offset:26624\n"
    "s_waitcnt lgkmcnt(8)\n"
    "v_lshlrev_b32 v32, 16, v144\n"
    "v_and_b32 v33, 0xffff0000, v144\n"
    "v_lshlrev_b32 v34, 16, v145\n"
    "v_and_b32 v35, 0xffff0000, v145\n"
    "v_lshlrev_b32 v36, 16, v146\n"
    "v_and_b32 v37, 0xffff0000, v146\n"
    "v_lshlrev_b32 v38, 16, v147\n"
    "v_and_b32 v39, 0xffff0000, v147\n"
    "v_lshlrev_b32 v40, 16, v148\n"
    "v_and_b32 v41, 0xffff0000, v148\n"
    "v_lshlrev_b32 v42, 16, v149\n"
    "v_and_b32 v43, 0xffff0000, v149\n"
    "v_lshlrev_b32 v44, 16, v150\n"
    "v_and_b32 v45, 0xffff0000, v150\n"
    "v_lshlrev_b32 v46, 16, v151\n"
    "v_and_b32 v47, 0xffff0000, v151\n"
    "v_mul_f32 v32, s66, v32\n"
    "v_mul_f32 v33, s66, v33\n"
    "v_mul_f32 v34, s66, v34\n"
    "v_mul_f32 v35, s66, v35\n"
    "v_mul_f32 v36, s66, v36\n"
    "v_mul_f32 v37, s66, v37\n"
    "v_mul_f32 v38, s66, v38\n"
    "v_mul_f32 v39, s66, v39\n"
    "v_mul_f32 v40, s66, v40\n"
    "v_mul_f32 v41, s66, v41\n"
    "v_mul_f32 v42, s66, v42\n"
    "v_mul_f32 v43, s66, v43\n"
    "v_mul_f32 v44, s66, v44\n"
    "v_mul_f32 v45, s66, v45\n"
    "v_mul_f32 v46, s66, v46\n"
    "v_mul_f32 v47, s66, v47\n"
    "v_cvt_pk_bf16_f32 v144, v32, v33\n"
    "v_cvt_pk_bf16_f32 v145, v34, v35\n"
    "v_cvt_pk_bf16_f32 v146, v36, v37\n"
    "v_cvt_pk_bf16_f32 v147, v38, v39\n"
    "v_cvt_pk_bf16_f32 v148, v40, v41\n"
    "v_cvt_pk_bf16_f32 v149, v42, v43\n"
    "v_cvt_pk_bf16_f32 v150, v44, v45\n"
    "v_cvt_pk_bf16_f32 v151, v46, v47\n"
    "s_waitcnt lgkmcnt(0)\n"
    "s_waitcnt vmcnt(6)\n"
    "ds_read_b128 v[176:179], v113 offset:2048\n"
    "ds_read_b128 v[180:183], v114 offset:2048\n"
    "s_add_u32 m0, s69, 0x20800\n"
    "s_nop 0\n"
    "buffer_load_dwordx4 v112, s[48:51], 0 offen nt lds\n"
    "v_add_u32 v112, 0x1000, v112\n"
    "s_add_u32 m0, s69, 0x20c00\n"
    "s_nop 0\n"
    "buffer_load_dwordx4 v112, s[48:51], 0 offen nt lds\n"
    "v_add_u32 v112, 0x1000, v112\n"
    "v_mfma_f32_16x16x32_bf16 v[0:3], v[80:83], v[144:147], v[0:3]\n"
    "v_mfma_f32_16x16x32_bf16 v[0:3], v[84:87], v[148:151], v[0:3]\n"
    "v_mfma_f32_16x16x32_bf16 v[48:51], v[88:91], v[144:147], v[48:51]\n"
    "v_mfma_f32_16x16x32_bf16 v[48:51], v[92:95], v[148:151], v[48:51]\n"
    "v_mfma_f32_16x16x32_bf16 v[16:19], v[96:99], v[144:147], v[16:19]\n"
    "v_mfma_f32_16x16x32_bf16 v[16:19], v[100:103], v[148:151], v[16:19]\n"
    "v_mfma_f32_16x16x32_bf16 v[64:67], v[104:107], v[144:147], v[64:67]\n"
    "v_mfma_f32_16x16x32_bf16 v[64:67], v[108:111], v[148:151], v[64:67]\n"
    "s_waitcnt lgkmcnt(0)\n"
    "v_lshlrev_b32 v32, 16, v176\n"
    "v_and_b32 v33, 0xffff0000, v176\n"
    "v_lshlrev_b32 v34, 16, v177\n"
    "v_and_b32 v35, 0xffff0000, v177\n"
    "v_lshlrev_b32 v36, 16, v178\n"
    "v_and_b32 v37, 0xffff0000, v178\n"
    "v_lshlrev_b32 v38, 16, v179\n"
    "v_and_b32 v39, 0xffff0000, v179\n"
    "v_lshlrev_b32 v40, 16, v180\n"
    "v_and_b32 v41, 0xffff0000, v180\n"
    "v_lshlrev_b32 v42, 16, v181\n"
    "v_and_b32 v43, 0xffff0000, v181\n"
    "v_lshlrev_b32 v44, 16, v182\n"
    "v_and_b32 v45, 0xffff0000, v182\n"
    "v_lshlrev_b32 v46, 16, v183\n"
    "v_and_b32 v47, 0xffff0000, v183\n"
    "v_mul_f32 v32, s66, v32\n"
    "v_mul_f32 v33, s66, v33\n"
    "v_mul_f32 v34, s66, v34\n"
    "v_mul_f32 v35, s66, v35\n"
    "v_mul_f32 v36, s66, v36\n"
    "v_mul_f32 v37, s66, v37\n"
    "v_mul_f32 v38, s66, v38\n"
    "v_mul_f32 v39, s66, v39\n"
    "v_mul_f32 v40, s66, v40\n"
    "v_mul_f32 v41, s66, v41\n"
    "v_mul_f32 v42, s66, v42\n"
    "v_mul_f32 v43, s66, v43\n"
    "v_mul_f32 v44, s66, v44\n"
    "v_mul_f32 v45, s66, v45\n"
    "v_mul_f32 v46, s66, v46\n"
    "v_mul_f32 v47, s66, v47\n"
    "v_cvt_pk_bf16_f32 v176, v32, v33\n"
    "v_cvt_pk_bf16_f32 v177, v34, v35\n"
    "v_cvt_pk_bf16_f32 v178, v36, v37\n"
    "v_cvt_pk_bf16_f32 v179, v38, v39\n"
    "v_cvt_pk_bf16_f32 v180, v40, v41\n"
    "v_cvt_pk_bf16_f32 v181, v42, v43\n"
    "v_cvt_pk_bf16_f32 v182, v44, v45\n"
    "v_cvt_pk_bf16_f32 v183, v46, v47\n"
    "v_mbcnt_lo_u32_b32 v124, -1, 0\n"
    "v_mbcnt_hi_u32_b32 v124, -1, v124\n"
    "s_getpc_b64 s[70:71]\n"
    "LPC1%=:\n"
    "s_add_u32 s70, s70, LSPEC%=-LPC1%=\n"
    "s_addc_u32 s71, s71, 0\n"
    "s_lshl_b32 s74, s20, 13\n"
    "v_lshl_add_u32 v125, v124, 7, s74\n"
    "v_min_u32 v125, LTAIL%=-LSPEC%=, v125\n"
    "global_load_dword v127, v125, s[70:71]\n"
    "s_getpc_b64 s[38:39]\n"
    "LPC2%=:\n"
    "s_add_u32 s38, s38, LTAIL%=-LPC2%=\n"
    "s_addc_u32 s39, s39, 0\n"
    "s_lshl_b32 s74, s20, 13\n"
    "v_lshl_add_u32 v126, v124, 7, s74\n"
    "v_min_u32 v126, LEND%=-LTAIL%=, v126\n"
    "s_waitcnt vmcnt(7)\n"
    "ds_read_b128 v[208:211], v113 offset:4096\n"
    "ds_read_b128 v[212:215], v114 offset:4096\n"
    "s_add_u32 m0, s69, 0x21000\n"
    "s_nop 0\n"
    "buffer_load_dwordx4 v112, s[48:51], 0 offen nt lds\n"
    "v_add_u32 v112, 0x1000, v112\n"
    "s_add_u32 m0, s69, 0x21400\n"
    "s_nop 0\n"
    "buffer_load_dwordx4 v112, s[48:51], 0 offen nt lds\n"
    "v_add_u32 v112, 0x1000, v112\n"
    "v_mfma_f32_16x16x32_bf16 v[4:7], v[80:83], v[176:179], v[4:7]\n"
    "v_mfma_f32_16x16x32_bf16 v[4:7], v[84:87], v[180:183], v[4:7]\n"
    "v_mfma_f32_16x16x32_bf16 v[52:55], v[88:91], v[176:179], v[52:55]\n"
    "v_mfma_f32_16x16x32_bf16 v[52:55], v[92:95], v[180:183], v[52:55]\n"
    "v_mfma_f32_16x16x32_bf16 v[20:23], v[96:99], v[176:179], v[20:23]\n"
    "v_mfma_f32_16x16x32_bf16 v[20:23], v[100:103], v[180:183], v[20:23]\n"
    "v_mfma_f32_16x16x32_bf16 v[68:71], v[104:107], v[176:179], v[68:71]\n"
    "v_mfma_f32_16x16x32_bf16 v[68:71], v[108:111], v[180:183], v[68:71]\n"
    "s_waitcnt lgkmcnt(0)\n"
    "v_lshlrev_b32 v32, 16, v208\n"
    "v_and_b32 v33, 0xffff0000, v208\n"
    "v_lshlrev_b32 v34, 16, v209\n"
    "v_and_b32 v35, 0xffff0000, v209\n"
    "v_lshlrev_b32 v36, 16, v210\n"
    "v_and_b32 v37, 0xffff0000, v210\n"
    "v_lshlrev_b32 v38, 16, v211\n"
    "v_and_b32 v39, 0xffff0000, v211\n"
    "v_lshlrev_b32 v40, 16, v212\n"
    "v_and_b32 v41, 0xffff0000, v212\n"
    "v_lshlrev_b32 v42, 16, v213\n"
    "v_and_b32 v43, 0xffff0000, v213\n"
    "v_lshlrev_b32 v44, 16, v214\n"
    "v_and_b32 v45, 0xffff0000, v214\n"
    "v_lshlrev_b32 v46, 16, v215\n"
    "v_and_b32 v47, 0xffff0000, v215\n"
    "v_mul_f32 v32, s66, v32\n"
    "v_mul_f32 v33, s66, v33\n"
    "v_mul_f32 v34, s66, v34\n"
    "v_mul_f32 v35, s66, v35\n"
    "v_mul_f32 v36, s66, v36\n"
    "v_mul_f32 v37, s66, v37\n"
    "v_mul_f32 v38, s66, v38\n"
    "v_mul_f32 v39, s66, v39\n"
    "v_mul_f32 v40, s66, v40\n"
    "v_mul_f32 v41, s66, v41\n"
    "v_mul_f32 v42, s66, v42\n"
    "v_mul_f32 v43, s66, v43\n"
    "v_mul_f32 v44, s66, v44\n"
    "v_mul_f32 v45, s66, v45\n"
    "v_mul_f32 v46, s66, v46\n"
    "v_mul_f32 v47, s66, v47\n"
    "v_cvt_pk_bf16_f32 v208, v32, v33\n"
    "v_cvt_pk_bf16_f32 v209, v34, v35\n"
    "v_cvt_pk_bf16_f32 v210, v36, v37\n"
    "v_cvt_pk_bf16_f32 v211, v38, v39\n"
    "v_cvt_pk_bf16_f32 v212, v40, v41\n"
    "v_cvt_pk_bf16_f32 v213, v42, v43\n"
    "v_cvt_pk_bf16_f32 v214, v44, v45\n"
    "v_cvt_pk_bf16_f32 v215, v46, v47\n"
    "s_waitcnt vmcnt(7)\n"
    "ds_read_b128 v[240:243], v113 offset:6144\n"
    "ds_read_b128 v[244:247], v114 offset:6144\n"
    "v_add_u32 v113, 0x8000, v113\n"
    "v_add_u32 v114, 0x8000, v114\n"
    "s_add_u32 m0, s69, 0x21800\n"
    "s_nop 0\n"
    "buffer_load_dwordx4 v112, s[48:51], 0 offen nt lds\n"
    "v_add_u32 v112, 0x1000, v112\n"
    "s_add_u32 m0, s69, 0x21c00\n"
    "s_nop 0\n"
    "buffer_load_dwordx4 v112, s[48:51], 0 offen nt lds\n"
    "v_add_u32 v112, 0xffff9080, v112\n"
    "v_mfma_f32_16x16x32_bf16 v[8:11], v[80:83], v[208:211], v[8:11]\n"
    "v_mfma_f32_16x16x32_bf16 v[8:11], v[84:87], v[212:215], v[8:11]\n"
    "v_mfma_f32_16x16x32_bf16 v[56:59], v[88:91], v[208:211], v[56:59]\n"
    "v_mfma_f32_16x16x32_bf16 v[56:59], v[92:95], v[212:215], v[56:59]\n"
    "v_mfma_f32_16x16x32_bf16 v[24:27], v[96:99], v[208:211], v[24:27]\n"
    "v_mfma_f32_16x16x32_bf16 v[24:27], v[100:103], v[212:215], v[24:27]\n"
    "v_mfma_f32_16x16x32_bf16 v[72:75], v[104:107], v[208:211], v[72:75]\n"
    "v_mfma_f32_16x16x32_bf16 v[72:75], v[108:111], v[212:215], v[72:75]\n"
    "s_waitcnt lgkmcnt(0)\n"
    "v_lshlrev_b32 v32, 16, v240\n"
    "v_and_b32 v33, 0xffff0000, v240\n"
    "v_lshlrev_b32 v34, 16, v241\n"
    "v_and_b32 v35, 0xffff0000, v241\n"
    "v_lshlrev_b32 v36, 16, v242\n"
    "v_and_b32 v37, 0xffff0000, v242\n"
    "v_lshlrev_b32 v38, 16, v243\n"
    "v_and_b32 v39, 0xffff0000, v243\n"
    "v_lshlrev_b32 v40, 16, v244\n"
    "v_and_b32 v41, 0xffff0000, v244\n"
    "v_lshlrev_b32 v42, 16, v245\n"
    "v_and_b32 v43, 0xffff0000, v245\n"
    "v_lshlrev_b32 v44, 16, v246\n"
    "v_and_b32 v45, 0xffff0000, v246\n"
    "v_lshlrev_b32 v46, 16, v247\n"
    "v_and_b32 v47, 0xffff0000, v247\n"
    "v_mul_f32 v32, s66, v32\n"
    "v_mul_f32 v33, s66, v33\n"
    "v_mul_f32 v34, s66, v34\n"
    "v_mul_f32 v35, s66, v35\n"
    "v_mul_f32 v36, s66, v36\n"
    "v_mul_f32 v37, s66, v37\n"
    "v_mul_f32 v38, s66, v38\n"
    "v_mul_f32 v39, s66, v39\n"
    "v_mul_f32 v40, s66, v40\n"
    "v_mul_f32 v41, s66, v41\n"
    "v_mul_f32 v42, s66, v42\n"
    "v_mul_f32 v43, s66, v43\n"
    "v_mul_f32 v44, s66, v44\n"
    "v_mul_f32 v45, s66, v45\n"
    "v_mul_f32 v46, s66, v46\n"
    "v_mul_f32 v47, s66, v47\n"
    "v_cvt_pk_bf16_f32 v240, v32, v33\n"
    "v_cvt_pk_bf16_f32 v241, v34, v35\n"
    "v_cvt_pk_bf16_f32 v242, v36, v37\n"
    "v_cvt_pk_bf16_f32 v243, v38, v39\n"
    "v_cvt_pk_bf16_f32 v244, v40, v41\n"
    "v_cvt_pk_bf16_f32 v245, v42, v43\n"
    "v_cvt_pk_bf16_f32 v246, v44, v45\n"
    "v_cvt_pk_bf16_f32 v247, v46, v47\n"
    "s_waitcnt vmcnt(7)\n"
    "ds_read_b128 v[152:155], v113 offset:0\n"
    "ds_read_b128 v[156:159], v114 offset:0\n"
    "v_add_u32 v123, 0x4000, v123\n"
    "s_add_u32 m0, s60, 0x8000\n"
    "s_nop 0\n"
    "buffer_load_dwordx4 v123, s[40:43], s56 offen offset:0 nt lds\n"
    "buffer_load_dwordx4 v123, s[40:43], s57 offen offset:1024 nt lds\n"
    "v_mfma_f32_16x16x32_bf16 v[12:15], v[80:83], v[240:243], v[12:15]\n"
    "v_mfma_f32_16x16x32_bf16 v[12:15], v[84:87], v[244:247], v[12:15]\n"
    "v_mfma_f32_16x16x32_bf16 v[60:63], v[88:91], v[240:243], v[60:63]\n"
    "v_mfma_f32_16x16x32_bf16 v[60:63], v[92:95], v[244:247], v[60:63]\n"
    "v_mfma_f32_16x16x32_bf16 v[28:31], v[96:99], v[240:243], v[28:31]\n"
    "v_mfma_f32_16x16x32_bf16 v[28:31], v[100:103], v[244:247], v[28:31]\n"
    "v_mfma_f32_16x16x32_bf16 v[76:79], v[104:107], v[240:243], v[76:79]\n"
    "v_mfma_f32_16x16x32_bf16 v[76:79], v[108:111], v[244:247], v[76:79]\n"
    "ds_read_b128 v[80:83], v120 offset:3072\n"
    "ds_read_b128 v[84:87], v121 offset:3072\n"
    "ds_read_b128 v[88:91], v120 offset:11264\n"
    "ds_read_b128 v[92:95], v121 offset:11264\n"
    "ds_read_b128 v[96:99], v120 offset:19456\n"
    "ds_read_b128 v[100:103], v121 offset:19456\n"
    "ds_read_b128 v[104:107], v120 offset:27648\n"
    "ds_read_b128 v[108:111], v121 offset:27648\n"
    "s_waitcnt lgkmcnt(8)\n"
    "v_lshlrev_b32 v32, 16, v152\n"
    "v_and_b32 v33, 0xffff0000, v152\n"
    "v_lshlrev_b32 v34, 16, v153\n"
    "v_and_b32 v35, 0xffff0000, v153\n"
    "v_lshlrev_b32 v36, 16, v154\n"
    "v_and_b32 v37, 0xffff0000, v154\n"
    "v_lshlrev_b32 v38, 16, v155\n"
    "v_and_b32 v39, 0xffff0000, v155\n"
    "v_lshlrev_b32 v40, 16, v156\n"
    "v_and_b32 v41, 0xffff0000, v156\n"
    "v_lshlrev_b32 v42, 16, v157\n"
    "v_and_b32 v43, 0xffff0000, v157\n"
    "v_lshlrev_b32 v44, 16, v158\n"
    "v_and_b32 v45, 0xffff0000, v158\n"
    "v_lshlrev_b32 v46, 16, v159\n"
    "v_and_b32 v47, 0xffff0000, v159\n"
    "v_mul_f32 v32, s66, v32\n"
    "v_mul_f32 v33, s66, v33\n"
    "v_mul_f32 v34, s66, v34\n"
    "v_mul_f32 v35, s66, v35\n"
    "v_mul_f32 v36, s66, v36\n"
    "v_mul_f32 v37, s66, v37\n"
    "v_mul_f32 v38, s66, v38\n"
    "v_mul_f32 v39, s66, v39\n"
    "v_mul_f32 v40, s66, v40\n"
    "v_mul_f32 v41, s66, v41\n"
    "v_mul_f32 v42, s66, v42\n"
    "v_mul_f32 v43, s66, v43\n"
    "v_mul_f32 v44, s66, v44\n"
    "v_mul_f32 v45, s66, v45\n"
    "v_mul_f32 v46, s66, v46\n"
    "v_mul_f32 v47, s66, v47\n"
    "v_cvt_pk_bf16_f32 v152, v32, v33\n"
    "v_cvt_pk_bf16_f32 v153, v34, v35\n"
    "v_cvt_pk_bf16_f32 v154, v36, v37\n"
    "v_cvt_pk_bf16_f32 v155, v38, v39\n"
    "v_cvt_pk_bf16_f32 v156, v40, v41\n"
    "v_cvt_pk_bf16_f32 v157, v42, v43\n"
    "v_cvt_pk_bf16_f32 v158, v44, v45\n"
    "v_cvt_pk_bf16_f32 v159, v46, v47\n"
    "s_waitcnt lgkmcnt(0)\n"
    "v_mfma_f32_16x16x32_bf16 v[0:3], v[80:83], v[152:155], v[0:3]\n"
    "v_mfma_f32_16x16x32_bf16 v[0:3], v[84:87], v[156:159], v[0:3]\n"
    "v_mfma_f32_16x16x32_bf16 v[48:51], v[88:91], v[152:155], v[48:51]\n"
    "v_mfma_f32_16x16x32_bf16 v[48:51], v[92:95], v[156:159], v[48:51]\n"
    "v_mfma_f32_16x16x32_bf16 v[16:19], v[96:99], v[152:155], v[16:19]\n"
    "v_mfma_f32_16x16x32_bf16 v[16:19], v[100:103], v[156:159], v[16:19]\n"
    "v_mfma_f32_16x16x32_bf16 v[64:67], v[104:107], v[152:155], v[64:67]\n"
    "v_mfma_f32_16x16x32_bf16 v[64:67], v[108:111], v[156:159], v[64:67]\n"
    "s_nop 1\n"
    "v_max3_f32 v124, v0, v1, v2\n"
    "s_nop 0\n"
    "v_max3_f32 v125, v3, v48, v49\n"
    "v_max3_f32 v124, v124, v50, v51\n"
    "v_max3_f32 v125, v125, v16, v17\n"
    "v_max3_f32 v124, v124, v18, v19\n"
    "v_max3_f32 v125, v125, v64, v65\n"
    "v_max3_f32 v124, v124, v66, v67\n"
    "v_max_f32 v124, v124, v125\n"
    "v_mov_b32 v125, v124\n"
    "s_nop 1\n"
    "v_permlane32_swap_b32 v124, v125\n"
    "v_max_f32 v124, v124, v125\n"
    "v_mov_b32 v125, v124\n"
    "s_nop 1\n"
    "v_permlane16_swap_b32 v124, v125\n"
    "v_max_f32 v124, v124, v125\n"
    "v_sub_f32 v0, v0, v124\n"
    "v_sub_f32 v1, v1, v124\n"
    "v_sub_f32 v2, v2, v124\n"
    "v_sub_f32 v3, v3, v124\n"
    "v_sub_f32 v48, v48, v124\n"
    "v_sub_f32 v49, v49, v124\n"
    "v_sub_f32 v50, v50, v124\n"
    "v_sub_f32 v51, v51, v124\n"
    "v_sub_f32 v16, v16, v124\n"
    "v_sub_f32 v17, v17, v124\n"
    "v_sub_f32 v18, v18, v124\n"
    "v_sub_f32 v19, v19, v124\n"
    "v_sub_f32 v64, v64, v124\n"
    "v_sub_f32 v65, v65, v124\n"
    "v_sub_f32 v66, v66, v124\n"
    "v_sub_f32 v67, v67, v124\n"
    "v_exp_f32 v0, v0\n"
    "v_exp_f32 v1, v1\n"
    "v_exp_f32 v2, v2\n"
    "v_exp_f32 v3, v3\n"
    "v_exp_f32 v48, v48\n"
    "v_exp_f32 v49, v49\n"
    "v_exp_f32 v50, v50\n"
    "v_exp_f32 v51, v51\n"
    "v_exp_f32 v16, v16\n"
    "v_exp_f32 v17, v17\n"
    "v_exp_f32 v18, v18\n"
    "v_exp_f32 v19, v19\n"
    "v_exp_f32 v64, v64\n"
    "v_exp_f32 v65, v65\n"
    "v_exp_f32 v66, v66\n"
    "v_exp_f32 v67, v67\n"
    "v_add_f32 v116, v116, v0\n"
    "v_add_f32 v116, v116, v1\n"
    "v_cvt_pk_bf16_f32 v0, v0, v1\n"
    "v_add_f32 v116, v116, v2\n"
    "v_add_f32 v116, v116, v3\n"
    "v_cvt_pk_bf16_f32 v1, v2, v3\n"
    "v_add_f32 v116, v116, v48\n"
    "v_add_f32 v116, v116, v49\n"
    "v_cvt_pk_bf16_f32 v2, v48, v49\n"
    "v_add_f32 v116, v116, v50\n"
    "v_add_f32 v116, v116, v51\n"
    "v_cvt_pk_bf16_f32 v3, v50, v51\n"
    "v_add_f32 v116, v116, v16\n"
    "v_add_f32 v116, v116, v17\n"
    "v_cvt_pk_bf16_f32 v16, v16, v17\n"
    "v_add_f32 v116, v116, v18\n"
    "v_add_f32 v116, v116, v19\n"
    "v_cvt_pk_bf16_f32 v17, v18, v19\n"
    "v_add_f32 v116, v116, v64\n"
    "v_add_f32 v116, v116, v65\n"
    "v_cvt_pk_bf16_f32 v18, v64, v65\n"
    "v_add_f32 v116, v116, v66\n"
    "v_add_f32 v116, v116, v67\n"
    "v_cvt_pk_bf16_f32 v19, v66, v67\n"
    "s_waitcnt vmcnt(7)\n"
    "ds_read_b128 v[184:187], v113 offset:2048\n"
    "ds_read_b128 v[188:191], v114 offset:2048\n"
    "s_add_u32 m0, s60, 0x8000\n"
    "s_nop 0\n"
    "buffer_load_dwordx4 v123, s[40:43], s58 offen offset:2048 nt lds\n"
    "buffer_load_dwordx4 v123, s[40:43], s59 offen offset:3072 nt lds\n"
    "s_waitcnt lgkmcnt(0)\n"
    "v_lshlrev_b32 v32, 16, v184\n"
    "v_and_b32 v33, 0xffff0000, v184\n"
    "v_lshlrev_b32 v34, 16, v185\n"
    "v_and_b32 v35, 0xffff0000, v185\n"
    "v_lshlrev_b32 v36, 16, v186\n"
    "v_and_b32 v37, 0xffff0000, v186\n"
    "v_lshlrev_b32 v38, 16, v187\n"
    "v_and_b32 v39, 0xffff0000, v187\n"
    "v_lshlrev_b32 v40, 16, v188\n"
    "v_and_b32 v41, 0xffff0000, v188\n"
    "v_lshlrev_b32 v42, 16, v189\n"
    "v_and_b32 v43, 0xffff0000, v189\n"
    "v_lshlrev_b32 v44, 16, v190\n"
    "v_and_b32 v45, 0xffff0000, v190\n"
    "v_lshlrev_b32 v46, 16, v191\n"
    "v_and_b32 v47, 0xffff0000, v191\n"
    "v_mul_f32 v32, s66, v32\n"
    "v_mul_f32 v33, s66, v33\n"
    "v_mul_f32 v34, s66, v34\n"
    "v_mul_f32 v35, s66, v35\n"
    "v_mul_f32 v36, s66, v36\n"
    "v_mul_f32 v37, s66, v37\n"
    "v_mul_f32 v38, s66, v38\n"
    "v_mul_f32 v39, s66, v39\n"
    "v_mul_f32 v40, s66, v40\n"
    "v_mul_f32 v41, s66, v41\n"
    "v_mul_f32 v42, s66, v42\n"
    "v_mul_f32 v43, s66, v43\n"
    "v_mul_f32 v44, s66, v44\n"
    "v_mul_f32 v45, s66, v45\n"
    "v_mul_f32 v46, s66, v46\n"
    "v_mul_f32 v47, s66, v47\n"
    "v_cvt_pk_bf16_f32 v184, v32, v33\n"
    "v_cvt_pk_bf16_f32 v185, v34, v35\n"
    "v_cvt_pk_bf16_f32 v186, v36, v37\n"
    "v_cvt_pk_bf16_f32 v187, v38, v39\n"
    "v_cvt_pk_bf16_f32 v188, v40, v41\n"
    "v_cvt_pk_bf16_f32 v189, v42, v43\n"
    "v_cvt_pk_bf16_f32 v190, v44, v45\n"
    "v_cvt_pk_bf16_f32 v191, v46, v47\n"
    "v_mfma_f32_16x16x32_bf16 v[4:7], v[80:83], v[184:187], v[4:7]\n"
    "s_nop 0\n"
    "v_mfma_f32_16x16x32_bf16 v[4:7], v[84:87], v[188:191], v[4:7]\n"
    "v_mfma_f32_16x16x32_bf16 v[52:55], v[88:91], v[184:187], v[52:55]\n"
    "v_mfma_f32_16x16x32_bf16 v[52:55], v[92:95], v[188:191], v[52:55]\n"
    "v_mfma_f32_16x16x32_bf16 v[20:23], v[96:99], v[184:187], v[20:23]\n"
    "v_mfma_f32_16x16x32_bf16 v[20:23], v[100:103], v[188:191], v[20:23]\n"
    "v_mfma_f32_16x16x32_bf16 v[68:71], v[104:107], v[184:187], v[68:71]\n"
    "v_mfma_f32_16x16x32_bf16 v[68:71], v[108:111], v[188:191], v[68:71]\n"
    "s_nop 1\n"
    "v_sub_f32 v4, v4, v124\n"
    "v_sub_f32 v5, v5, v124\n"
    "v_sub_f32 v6, v6, v124\n"
    "v_sub_f32 v7, v7, v124\n"
    "v_sub_f32 v52, v52, v124\n"
    "v_sub_f32 v53, v53, v124\n"
    "v_sub_f32 v54, v54, v124\n"
    "v_sub_f32 v55, v55, v124\n"
    "v_sub_f32 v20, v20, v124\n"
    "v_sub_f32 v21, v21, v124\n"
    "v_sub_f32 v22, v22, v124\n"
    "v_sub_f32 v23, v23, v124\n"
    "v_sub_f32 v68, v68, v124\n"
    "v_sub_f32 v69, v69, v124\n"
    "v_sub_f32 v70, v70, v124\n"
    "v_sub_f32 v71, v71, v124\n"
    "v_exp_f32 v4, v4\n"
    "v_exp_f32 v5, v5\n"
    "v_exp_f32 v6, v6\n"
    "v_exp_f32 v7, v7\n"
    "v_exp_f32 v52, v52\n"
    "v_exp_f32 v53, v53\n"
    "v_exp_f32 v54, v54\n"
    "v_exp_f32 v55, v55\n"
    "v_exp_f32 v20, v20\n"
    "v_exp_f32 v21, v21\n"
    "v_exp_f32 v22, v22\n"
    "v_exp_f32 v23, v23\n"
    "v_exp_f32 v68, v68\n"
    "v_exp_f32 v69, v69\n"
    "v_exp_f32 v70, v70\n"
    "v_exp_f32 v71, v71\n"
    "v_add_f32 v117, v117, v4\n"
    "v_add_f32 v117, v117, v5\n"
    "v_cvt_pk_bf16_f32 v4, v4, v5\n"
    "v_add_f32 v117, v117, v6\n"
    "v_add_f32 v117, v117, v7\n"
    "v_cvt_pk_bf16_f32 v5, v6, v7\n"
    "v_add_f32 v117, v117, v52\n"
    "v_add_f32 v117, v117, v53\n"
    "v_cvt_pk_bf16_f32 v6, v52, v53\n"
    "v_add_f32 v117, v117, v54\n"
    "v_add_f32 v117, v117, v55\n"
    "v_cvt_pk_bf16_f32 v7, v54, v55\n"
    "v_add_f32 v117, v117, v20\n"
    "v_add_f32 v117, v117, v21\n"
    "v_cvt_pk_bf16_f32 v20, v20, v21\n"
    "v_add_f32 v117, v117, v22\n"
    "v_add_f32 v117, v117, v23\n"
    "v_cvt_pk_bf16_f32 v21, v22, v23\n"
    "v_add_f32 v117, v117, v68\n"
    "v_add_f32 v117, v117, v69\n"
    "v_cvt_pk_bf16_f32 v22, v68, v69\n"
    "v_add_f32 v117, v117, v70\n"
    "v_add_f32 v117, v117, v71\n"
    "v_cvt_pk_bf16_f32 v23, v70, v71\n"
    "s_waitcnt vmcnt(6)\n"
    "ds_read_b128 v[216:219], v113 offset:4096\n"
    "ds_read_b128 v[220:223], v114 offset:4096\n"
    "v_add_u32 v123, 0xffffc000, v123\n"
    "s_add_u32 m0, s61, 0x0\n"
    "s_nop 0\n"
    "buffer_load_dwordx4 v123, s[44:47], s56 offen offset:0 nt lds\n"
    "buffer_load_dwordx4 v123, s[44:47], s57 offen offset:1024 nt lds\n"
    "s_waitcnt lgkmcnt(0)\n"
    "v_lshlrev_b32 v32, 16, v216\n"
    "v_and_b32 v33, 0xffff0000, v216\n"
    "v_lshlrev_b32 v34, 16, v217\n"
    "v_and_b32 v35, 0xffff0000, v217\n"
    "v_lshlrev_b32 v36, 16, v218\n"
    "v_and_b32 v37, 0xffff0000, v218\n"
    "v_lshlrev_b32 v38, 16, v219\n"
    "v_and_b32 v39, 0xffff0000, v219\n"
    "v_lshlrev_b32 v40, 16, v220\n"
    "v_and_b32 v41, 0xffff0000, v220\n"
    "v_lshlrev_b32 v42, 16, v221\n"
    "v_and_b32 v43, 0xffff0000, v221\n"
    "v_lshlrev_b32 v44, 16, v222\n"
    "v_and_b32 v45, 0xffff0000, v222\n"
    "v_lshlrev_b32 v46, 16, v223\n"
    "v_and_b32 v47, 0xffff0000, v223\n"
    "v_mul_f32 v32, s66, v32\n"
    "v_mul_f32 v33, s66, v33\n"
    "v_mul_f32 v34, s66, v34\n"
    "v_mul_f32 v35, s66, v35\n"
    "v_mul_f32 v36, s66, v36\n"
    "v_mul_f32 v37, s66, v37\n"
    "v_mul_f32 v38, s66, v38\n"
    "v_mul_f32 v39, s66, v39\n"
    "v_mul_f32 v40, s66, v40\n"
    "v_mul_f32 v41, s66, v41\n"
    "v_mul_f32 v42, s66, v42\n"
    "v_mul_f32 v43, s66, v43\n"
    "v_mul_f32 v44, s66, v44\n"
    "v_mul_f32 v45, s66, v45\n"
    "v_mul_f32 v46, s66, v46\n"
    "v_mul_f32 v47, s66, v47\n"
    "v_cvt_pk_bf16_f32 v216, v32, v33\n"
    "v_cvt_pk_bf16_f32 v217, v34, v35\n"
    "v_cvt_pk_bf16_f32 v218, v36, v37\n"
    "v_cvt_pk_bf16_f32 v219, v38, v39\n"
    "v_cvt_pk_bf16_f32 v220, v40, v41\n"
    "v_cvt_pk_bf16_f32 v221, v42, v43\n"
    "v_cvt_pk_bf16_f32 v222, v44, v45\n"
    "v_cvt_pk_bf16_f32 v223, v46, v47\n"
    "v_mfma_f32_16x16x32_bf16 v[8:11], v[80:83], v[216:219], v[8:11]\n"
    "s_nop 0\n"
    "v_mfma_f32_16x16x32_bf16 v[8:11], v[84:87], v[220:223], v[8:11]\n"
    "v_mfma_f32_16x16x32_bf16 v[56:59], v[88:91], v[216:219], v[56:59]\n"
    "v_mfma_f32_16x16x32_bf16 v[56:59], v[92:95], v[220:223], v[56:59]\n"
    "v_mfma_f32_16x16x32_bf16 v[24:27], v[96:99], v[216:219], v[24:27]\n"
    "v_mfma_f32_16x16x32_bf16 v[24:27], v[100:103], v[220:223], v[24:27]\n"
    "v_mfma_f32_16x16x32_bf16 v[72:75], v[104:107], v[216:219], v[72:75]\n"
    "v_mfma_f32_16x16x32_bf16 v[72:75], v[108:111], v[220:223], v[72:75]\n"
    "s_nop 1\n"
    "v_sub_f32 v8, v8, v124\n"
    "v_sub_f32 v9, v9, v124\n"
    "v_sub_f32 v10, v10, v124\n"
    "v_sub_f32 v11, v11, v124\n"
    "v_sub_f32 v56, v56, v124\n"
    "v_sub_f32 v57, v57, v124\n"
    "v_sub_f32 v58, v58, v124\n"
    "v_sub_f32 v59, v59, v124\n"
    "v_sub_f32 v24, v24, v124\n"
    "v_sub_f32 v25, v25, v124\n"
    "v_sub_f32 v26, v26, v124\n"
    "v_sub_f32 v27, v27, v124\n"
    "v_sub_f32 v72, v72, v124\n"
    "v_sub_f32 v73, v73, v124\n"
    "v_sub_f32 v74, v74, v124\n"
    "v_sub_f32 v75, v75, v124\n"
    "v_exp_f32 v8, v8\n"
    "v_exp_f32 v9, v9\n"
    "v_exp_f32 v10, v10\n"
    "v_exp_f32 v11, v11\n"
    "v_exp_f32 v56, v56\n"
    "v_exp_f32 v57, v57\n"
    "v_exp_f32 v58, v58\n"
    "v_exp_f32 v59, v59\n"
    "v_exp_f32 v24, v24\n"
    "v_exp_f32 v25, v25\n"
    "v_exp_f32 v26, v26\n"
    "v_exp_f32 v27, v27\n"
    "v_add_f32 v118, v118, v8\n"
    "v_add_f32 v118, v118, v9\n"
    "v_cvt_pk_bf16_f32 v8, v8, v9\n"
    "v_add_f32 v118, v118, v10\n"
    "v_add_f32 v118, v118, v11\n"
    "v_cvt_pk_bf16_f32 v9, v10, v11\n"
    "v_add_f32 v118, v118, v56\n"
    "v_add_f32 v118, v118, v57\n"
    "v_cvt_pk_bf16_f32 v10, v56, v57\n"
    "v_add_f32 v118, v118, v58\n"
    "v_add_f32 v118, v118, v59\n"
    "v_cvt_pk_bf16_f32 v11, v58, v59\n"
    "v_add_f32 v118, v118, v24\n"
    "v_add_f32 v118, v118, v25\n"
    "v_cvt_pk_bf16_f32 v24, v24, v25\n"
    "v_add_f32 v118, v118, v26\n"
    "v_add_f32 v118, v118, v27\n"
    "v_cvt_pk_bf16_f32 v25, v26, v27\n"
    "s_waitcnt vmcnt(6)\n"
    "ds_read_b128 v[248:251], v113 offset:6144\n"
    "ds_read_b128 v[252:255], v114 offset:6144\n"
    "s_add_u32 m0, s61, 0x0\n"
    "s_nop 0\n"
    "buffer_load_dwordx4 v123, s[44:47], s58 offen offset:2048 nt lds\n"
    "buffer_load_dwordx4 v123, s[44:47], s59 offen offset:3072 nt lds\n"
    "v_add_u32 v123, 0x4000, v123\n"
    "s_add_u32 m0, s61, 0x4000\n"
    "s_nop 0\n"
    "buffer_load_dwordx4 v123, s[44:47], s56 offen offset:0 nt lds\n"
    "buffer_load_dwordx4 v123, s[44:47], s57 offen offset:1024 nt lds\n"
    "s_add_u32 m0, s61, 0x4000\n"
    "s_nop 0\n"
    "buffer_load_dwordx4 v123, s[44:47], s58 offen offset:2048 nt lds\n"
    "buffer_load_dwordx4 v123, s[44:47], s59 offen offset:3072 nt lds\n"
    "v_sub_f32 v112, 0, v124\n"
    "v_sub_f32 v113, 0, v124\n"
    "v_sub_f32 v114, 0, v124\n"
    "v_sub_f32 v115, 0, v124\n"
    "s_waitcnt lgkmcnt(0)\n"
    "v_lshlrev_b32 v32, 16, v248\n"
    "v_and_b32 v33, 0xffff0000, v248\n"
    "v_lshlrev_b32 v34, 16, v249\n"
    "v_and_b32 v35, 0xffff0000, v249\n"
    "v_lshlrev_b32 v36, 16, v250\n"
    "v_and_b32 v37, 0xffff0000, v250\n"
    "v_lshlrev_b32 v38, 16, v251\n"
    "v_and_b32 v39, 0xffff0000, v251\n"
    "v_lshlrev_b32 v40, 16, v252\n"
    "v_and_b32 v41, 0xffff0000, v252\n"
    "v_lshlrev_b32 v42, 16, v253\n"
    "v_and_b32 v43, 0xffff0000, v253\n"
    "v_lshlrev_b32 v44, 16, v254\n"
    "v_and_b32 v45, 0xffff0000, v254\n"
    "v_lshlrev_b32 v46, 16, v255\n"
    "v_and_b32 v47, 0xffff0000, v255\n"
    "v_mul_f32 v32, s66, v32\n"
    "v_mul_f32 v33, s66, v33\n"
    "v_mul_f32 v34, s66, v34\n"
    "v_mul_f32 v35, s66, v35\n"
    "v_mul_f32 v36, s66, v36\n"
    "v_mul_f32 v37, s66, v37\n"
    "v_mul_f32 v38, s66, v38\n"
    "v_mul_f32 v39, s66, v39\n"
    "v_mul_f32 v40, s66, v40\n"
    "v_mul_f32 v41, s66, v41\n"
    "v_mul_f32 v42, s66, v42\n"
    "v_mul_f32 v43, s66, v43\n"
    "v_mul_f32 v44, s66, v44\n"
    "v_mul_f32 v45, s66, v45\n"
    "v_mul_f32 v46, s66, v46\n"
    "v_mul_f32 v47, s66, v47\n"
    "v_cvt_pk_bf16_f32 v248, v32, v33\n"
    "v_cvt_pk_bf16_f32 v249, v34, v35\n"
    "v_cvt_pk_bf16_f32 v250, v36, v37\n"
    "v_cvt_pk_bf16_f32 v251, v38, v39\n"
    "v_cvt_pk_bf16_f32 v252, v40, v41\n"
    "v_cvt_pk_bf16_f32 v253, v42, v43\n"
    "v_cvt_pk_bf16_f32 v254, v44, v45\n"
    "v_cvt_pk_bf16_f32 v255, v46, v47\n"
    "v_mfma_f32_16x16x32_bf16 v[12:15], v[80:83], v[248:251], v[12:15]\n"
    "s_nop 0\n"
    "v_mfma_f32_16x16x32_bf16 v[12:15], v[84:87], v[252:255], v[12:15]\n"
    "v_mfma_f32_16x16x32_bf16 v[60:63], v[88:91], v[248:251], v[60:63]\n"
    "v_mfma_f32_16x16x32_bf16 v[60:63], v[92:95], v[252:255], v[60:63]\n"
    "v_mfma_f32_16x16x32_bf16 v[28:31], v[96:99], v[248:251], v[28:31]\n"
    "v_mfma_f32_16x16x32_bf16 v[28:31], v[100:103], v[252:255], v[28:31]\n"
    "v_mfma_f32_16x16x32_bf16 v[76:79], v[104:107], v[248:251], v[76:79]\n"
    "v_mfma_f32_16x16x32_bf16 v[76:79], v[108:111], v[252:255], v[76:79]\n"
    "s_waitcnt vmcnt(8)\n"
    "s_barrier\n"
    "ds_read_b128 v[80:83], v120 offset:32768\n"
    "ds_read_b128 v[84:87], v121 offset:32768\n"
    "ds_read_b128 v[88:91], v120 offset:33792\n"
    "ds_read_b128 v[92:95], v121 offset:33792\n"
    "ds_read_b128 v[96:99], v120 offset:34816\n"
    "ds_read_b128 v[100:103], v121 offset:34816\n"
    "v_sub_f32 v76, v76, v124\n"
    "v_sub_f32 v77, v77, v124\n"
    "v_sub_f32 v78, v78, v124\n"
    "v_sub_f32 v79, v79, v124\n"
    "v_sub_f32 v28, v28, v124\n"
    "v_sub_f32 v29, v29, v124\n"
    "v_sub_f32 v30, v30, v124\n"
    "v_sub_f32 v31, v31, v124\n"
    "v_exp_f32 v28, v28\n"
    "v_exp_f32 v29, v29\n"
    "v_exp_f32 v30, v30\n"
    "v_exp_f32 v31, v31\n"
    "v_add_f32 v119, v119, v28\n"
    "v_add_f32 v119, v119, v29\n"
    "v_cvt_pk_bf16_f32 v28, v28, v29\n"
    "v_add_f32 v119, v119, v30\n"
    "v_add_f32 v119, v119, v31\n"
    "v_cvt_pk_bf16_f32 v29, v30, v31\n"
    "s_waitcnt lgkmcnt(2)\n"
    "LSPEC%=:\n"
    "v_mfma_f32_16x16x32_bf16 v[32:35], v[80:83], v[128:131], v[112:115]\n"
    "v_exp_f32 v72, v72\n"
    "v_mfma_f32_16x16x32_bf16 v[32:35], v[84:87], v[132:135], v[32:35]\n"
    "v_exp_f32 v73, v73\n"
    "v_mfma_f32_16x16x32_bf16 v[32:35], v[88:91], v[136:139], v[32:35]\n"
    "ds_read_b128 v[104:107], v120 offset:35840\n"
    "v_add_f32 v118, v118, v72\n"
    "v_mfma_f32_16x16x32_bf16 v[32:35], v[92:95], v[140:143], v[32:35]\n"
    "ds_read_b128 v[108:111], v121 offset:35840\n"
    "v_add_f32 v118, v118, v73\n"
    "v_mfma_f32_16x16x32_bf16 v[36:39], v[92:95], v[172:175], v[112:115]\n"
    "v_cvt_pk_bf16_f32 v26, v72, v73\n"
    "v_add_u32 v123, 0x4000, v123\n"
    "v_mfma_f32_16x16x32_bf16 v[36:39], v[88:91], v[168:171], v[36:39]\n"
    "s_add_u32 m0, s60, 0xc000\n"
    "v_sub_f32 v12, v12, v124\n"
    "v_mfma_f32_16x16x32_bf16 v[36:39], v[84:87], v[164:167], v[36:39]\n"
    "buffer_load_dwordx4 v123, s[40:43], s56 offen offset:0 nt lds\n"
    "v_mfma_f32_16x16x32_bf16 v[36:39], v[80:83], v[160:163], v[36:39]\n"
    "buffer_load_dwordx4 v123, s[40:43], s57 offen offset:1024 nt lds\n"
    "v_mfma_f32_16x16x32_bf16 v[40:43], v[80:83], v[192:195], v[112:115]\n"
    "v_exp_f32 v74, v74\n"
    "v_mfma_f32_16x16x32_bf16 v[40:43], v[84:87], v[196:199], v[40:43]\n"
    "v_exp_f32 v75, v75\n"
    "v_mfma_f32_16x16x32_bf16 v[40:43], v[88:91], v[200:203], v[40:43]\n"
    "v_add_f32 v118, v118, v74\n"
    "v_sub_f32 v13, v13, v124\n"
    "v_mfma_f32_16x16x32_bf16 v[40:43], v[92:95], v[204:207], v[40:43]\n"
    "v_add_f32 v118, v118, v75\n"
    "v_sub_f32 v14, v14, v124\n"
    "v_mfma_f32_16x16x32_bf16 v[44:47], v[92:95], v[236:239], v[112:115]\n"
    "v_cvt_pk_bf16_f32 v27, v74, v75\n"
    "v_sub_f32 v15, v15, v124\n"
    "v_mfma_f32_16x16x32_bf16 v[44:47], v[88:91], v[232:235], v[44:47]\n"
    "buffer_load_dwordx4 v123, s[40:43], s58 offen offset:2048 nt lds\n"
    "v_mfma_f32_16x16x32_bf16 v[44:47], v[84:87], v[228:231], v[44:47]\n"
    "v_sub_f32 v60, v60, v124\n"
    "v_sub_f32 v61, v61, v124\n"
    "v_mfma_f32_16x16x32_bf16 v[44:47], v[80:83], v[224:227], v[44:47]\n"
    "v_sub_f32 v62, v62, v124\n"
    "s_waitcnt lgkmcnt(0)\n"
    "v_mfma_f32_16x16x32_bf16 v[32:35], v[96:99], v[144:147], v[32:35]\n"
    "v_exp_f32 v76, v76\n"
    "v_mfma_f32_16x16x32_bf16 v[32:35], v[100:103], v[148:151], v[32:35]\n"
    "v_exp_f32 v77, v77\n"
    "v_mfma_f32_16x16x32_bf16 v[32:35], v[104:107], v[152:155], v[32:35]\n"
    "ds_read_b128 v[80:83], v120 offset:40960\n"
    "v_add_f32 v119, v119, v76\n"
    "v_mfma_f32_16x16x32_bf16 v[32:35], v[108:111], v[156:159], v[32:35]\n"
    "ds_read_b128 v[84:87], v121 offset:40960\n"
    "v_add_f32 v119, v119, v77\n"
    "v_mfma_f32_16x16x32_bf16 v[36:39], v[108:111], v[188:191], v[36:39]\n"
    "ds_read_b128 v[88:91], v120 offset:41984\n"
    "v_cvt_pk_bf16_f32 v30, v76, v77\n"
    "v_mfma_f32_16x16x32_bf16 v[36:39], v[104:107], v[184:187], v[36:39]\n"
    "ds_read_b128 v[92:95], v121 offset:41984\n"
    "v_sub_f32 v63, v63, v124\n"
    "v_mfma_f32_16x16x32_bf16 v[36:39], v[100:103], v[180:183], v[36:39]\n"
    "buffer_load_dwordx4 v123, s[40:43], s59 offen offset:3072 nt lds\n"
    "v_mfma_f32_16x16x32_bf16 v[36:39], v[96:99], v[176:179], v[36:39]\n"
    "v_exp_f32 v12, v12\n"
    "v_mfma_f32_16x16x32_bf16 v[40:43], v[96:99], v[208:211], v[40:43]\n"
    "v_exp_f32 v78, v78\n"
    "v_mfma_f32_16x16x32_bf16 v[40:43], v[100:103], v[212:215], v[40:43]\n"
    "v_exp_f32 v79, v79\n"
    "v_mfma_f32_16x16x32_bf16 v[40:43], v[104:107], v[216:219], v[40:43]\n"
    "v_add_f32 v119, v119, v78\n"
    "v_mfma_f32_16x16x32_bf16 v[40:43], v[108:111], v[220:223], v[40:43]\n"
    "v_add_f32 v119, v119, v79\n"
    "v_mfma_f32_16x16x32_bf16 v[44:47], v[108:111], v[252:255], v[44:47]\n"
    "v_cvt_pk_bf16_f32 v31, v78, v79\n"
    "v_mfma_f32_16x16x32_bf16 v[44:47], v[104:107], v[248:251], v[44:47]\n"
    "v_exp_f32 v13, v13\n"
    "v_mfma_f32_16x16x32_bf16 v[44:47], v[100:103], v[244:247], v[44:47]\n"
    "v_exp_f32 v14, v14\n"
    "v_mfma_f32_16x16x32_bf16 v[44:47], v[96:99], v[240:243], v[44:47]\n"
    "s_add_u32 m0, s61, 0x8000\n"
    "s_waitcnt lgkmcnt(0)\n"
    "v_mfma_f32_16x16x32_bf16 v[64:67], v[80:83], v[128:131], v[112:115]\n"
    "v_exp_f32 v32, v32\n"
    "v_mfma_f32_16x16x32_bf16 v[64:67], v[84:87], v[132:135], v[64:67]\n"
    "v_exp_f32 v33, v33\n"
    "v_mfma_f32_16x16x32_bf16 v[64:67], v[88:91], v[136:139], v[64:67]\n"
    "ds_read_b128 v[96:99], v120 offset:43008\n"
    "v_add_f32 v116, v116, v32\n"
    "v_mfma_f32_16x16x32_bf16 v[64:67], v[92:95], v[140:143], v[64:67]\n"
    "ds_read_b128 v[100:103], v121 offset:43008\n"
    "v_add_f32 v116, v116, v33\n"
    "v_mfma_f32_16x16x32_bf16 v[68:71], v[92:95], v[172:175], v[112:115]\n"
    "ds_read_b128 v[104:107], v120 offset:44032\n"
    "v_cvt_pk_bf16_f32 v32, v32, v33\n"
    "v_mfma_f32_16x16x32_bf16 v[68:71], v[88:91], v[168:171], v[68:71]\n"
    "ds_read_b128 v[108:111], v121 offset:44032\n"
    "v_mfma_f32_16x16x32_bf16 v[68:71], v[84:87], v[164:167], v[68:71]\n"
    "buffer_load_dwordx4 v123, s[44:47], s56 offen offset:0 nt lds\n"
    "v_mfma_f32_16x16x32_bf16 v[68:71], v[80:83], v[160:163], v[68:71]\n"
    "buffer_load_dwordx4 v123, s[44:47], s57 offen offset:1024 nt lds\n"
    "v_mfma_f32_16x16x32_bf16 v[72:75], v[80:83], v[192:195], v[112:115]\n"
    "v_exp_f32 v34, v34\n"
    "v_mfma_f32_16x16x32_bf16 v[72:75], v[84:87], v[196:199], v[72:75]\n"
    "v_exp_f32 v35, v35\n"
    "v_mfma_f32_16x16x32_bf16 v[72:75], v[88:91], v[200:203], v[72:75]\n"
    "v_add_f32 v116, v116, v34\n"
    "v_mfma_f32_16x16x32_bf16 v[72:75], v[92:95], v[204:207], v[72:75]\n"
    "v_add_f32 v116, v116, v35\n"
    "v_mfma_f32_16x16x32_bf16 v[76:79], v[92:95], v[236:239], v[112:115]\n"
    "v_cvt_pk_bf16_f32 v33, v34, v35\n"
    "v_mfma_f32_16x16x32_bf16 v[76:79], v[88:91], v[232:235], v[76:79]\n"
    "buffer_load_dwordx4 v123, s[44:47], s58 offen offset:2048 nt lds\n"
    "v_mfma_f32_16x16x32_bf16 v[76:79], v[84:87], v[228:231], v[76:79]\n"
    "v_exp_f32 v15, v15\n"
    "v_mfma_f32_16x16x32_bf16 v[76:79], v[80:83], v[224:227], v[76:79]\n"
    "s_waitcnt vmcnt(7) lgkmcnt(0)\n"
    "s_barrier\n"
    "v_mfma_f32_16x16x32_bf16 v[64:67], v[96:99], v[144:147], v[64:67]\n"
    "v_exp_f32 v36, v36\n"
    "v_mfma_f32_16x16x32_bf16 v[64:67], v[100:103], v[148:151], v[64:67]\n"
    "v_exp_f32 v37, v37\n"
    "v_mfma_f32_16x16x32_bf16 v[64:67], v[104:107], v[152:155], v[64:67]\n"
    "ds_read_b64_tr_b16 v[80:81], v122 offset:0\n"
    "v_add_f32 v117, v117, v36\n"
    "v_mfma_f32_16x16x32_bf16 v[64:67], v[108:111], v[156:159], v[64:67]\n"
    "ds_read_b64_tr_b16 v[82:83], v122 offset:8192\n"
    "v_add_f32 v117, v117, v37\n"
    "v_mfma_f32_16x16x32_bf16 v[68:71], v[108:111], v[188:191], v[68:71]\n"
    "ds_read_b64_tr_b16 v[84:85], v122 offset:16384\n"
    "v_cvt_pk_bf16_f32 v36, v36, v37\n"
    "v_mfma_f32_16x16x32_bf16 v[68:71], v[104:107], v[184:187], v[68:71]\n"
    "ds_read_b64_tr_b16 v[86:87], v122 offset:24576\n"
    "v_mfma_f32_16x16x32_bf16 v[68:71], v[100:103], v[180:183], v[68:71]\n"
    "buffer_load_dwordx4 v123, s[44:47], s59 offen offset:3072 nt lds\n"
    "v_mfma_f32_16x16x32_bf16 v[68:71], v[96:99], v[176:179], v[68:71]\n"
    "v_exp_f32 v60, v60\n"
    "v_mfma_f32_16x16x32_bf16 v[72:75], v[96:99], v[208:211], v[72:75]\n"
    "v_exp_f32 v38, v38\n"
    "v_mfma_f32_16x16x32_bf16 v[72:75], v[100:103], v[212:215], v[72:75]\n"
    "v_exp_f32 v39, v39\n"
    "v_mfma_f32_16x16x32_bf16 v[72:75], v[104:107], v[216:219], v[72:75]\n"
    "ds_read_b64_tr_b16 v[88:89], v122 offset:1024\n"
    "v_add_f32 v117, v117, v38\n"
    "v_mfma_f32_16x16x32_bf16 v[72:75], v[108:111], v[220:223], v[72:75]\n"
    "ds_read_b64_tr_b16 v[90:91], v122 offset:9216\n"
    "v_add_f32 v117, v117, v39\n"
    "v_mfma_f32_16x16x32_bf16 v[76:79], v[108:111], v[252:255], v[76:79]\n"
    "ds_read_b64_tr_b16 v[92:93], v122 offset:17408\n"
    "v_cvt_pk_bf16_f32 v37, v38, v39\n"
    "v_mfma_f32_16x16x32_bf16 v[76:79], v[104:107], v[248:251], v[76:79]\n"
    "ds_read_b64_tr_b16 v[94:95], v122 offset:25600\n"
    "v_mfma_f32_16x16x32_bf16 v[76:79], v[100:103], v[244:247], v[76:79]\n"
    "v_add_u32 v123, 0x4000, v123\n"
    "s_add_u32 m0, s60, 0x0\n"
    "v_mfma_f32_16x16x32_bf16 v[76:79], v[96:99], v[240:243], v[76:79]\n"
    "v_exp_f32 v61, v61\n"
    "v_exp_f32 v62, v62\n"
    "v_exp_f32 v63, v63\n"
    "v_add_f32 v119, v119, v12\n"
    "v_add_f32 v119, v119, v13\n"
    "v_cvt_pk_bf16_f32 v12, v12, v13\n"
    "v_add_f32 v119, v119, v14\n"
    "v_add_f32 v119, v119, v15\n"
    "v_cvt_pk_bf16_f32 v13, v14, v15\n"
    "v_add_f32 v119, v119, v60\n"
    "v_add_f32 v119, v119, v61\n"
    "v_cvt_pk_bf16_f32 v14, v60, v61\n"
    "v_add_f32 v119, v119, v62\n"
    "v_add_f32 v119, v119, v63\n"
    "v_cvt_pk_bf16_f32 v15, v62, v63\n"
    "s_waitcnt lgkmcnt(4)\n"
    "v_mfma_f32_16x16x32_bf16 a[0:3], v[80:83], v[0:3], 0\n"
    "v_exp_f32 v40, v40\n"
    "v_mfma_f32_16x16x32_bf16 a[0:3], v[84:87], v[16:19], a[0:3]\n"
    "v_exp_f32 v41, v41\n"
    "v_mfma_f32_16x16x32_bf16 a[4:7], v[84:87], v[20:23], 0\n"
    "ds_read_b64_tr_b16 v[96:97], v122 offset:2048\n"
    "v_add_f32 v118, v118, v40\n"
    "v_mfma_f32_16x16x32_bf16 a[4:7], v[80:83], v[4:7], a[4:7]\n"
    "ds_read_b64_tr_b16 v[98:99], v122 offset:10240\n"
    "v_add_f32 v118, v118, v41\n"
    "v_mfma_f32_16x16x32_bf16 a[8:11], v[80:83], v[8:11], 0\n"
    "ds_read_b64_tr_b16 v[100:101], v122 offset:18432\n"
    "v_cvt_pk_bf16_f32 v40, v40, v41\n"
    "v_mfma_f32_16x16x32_bf16 a[8:11], v[84:87], v[24:27], a[8:11]\n"
    "ds_read_b64_tr_b16 v[102:103], v122 offset:26624\n"
    "v_mfma_f32_16x16x32_bf16 a[12:15], v[84:87], v[28:31], 0\n"
    "buffer_load_dwordx4 v123, s[40:43], s56 offen offset:0 nt lds\n"
    "v_mfma_f32_16x16x32_bf16 a[12:15], v[80:83], v[12:15], a[12:15]\n"
    "s_waitcnt lgkmcnt(4)\n"
    "v_mfma_f32_16x16x32_bf16 a[64:67], v[88:91], v[0:3], 0\n"
    "v_exp_f32 v42, v42\n"
    "v_mfma_f32_16x16x32_bf16 a[64:67], v[92:95], v[16:19], a[64:67]\n"
    "v_exp_f32 v43, v43\n"
    "v_mfma_f32_16x16x32_bf16 a[68:71], v[92:95], v[20:23], 0\n"
    "ds_read_b64_tr_b16 v[104:105], v122 offset:3072\n"
    "v_add_f32 v118, v118, v42\n"
    "v_mfma_f32_16x16x32_bf16 a[68:71], v[88:91], v[4:7], a[68:71]\n"
    "ds_read_b64_tr_b16 v[106:107], v122 offset:11264\n"
    "v_add_f32 v118, v118, v43\n"
    "v_mfma_f32_16x16x32_bf16 a[72:75], v[88:91], v[8:11], 0\n"
    "ds_read_b64_tr_b16 v[108:109], v122 offset:19456\n"
    "v_cvt_pk_bf16_f32 v41, v42, v43\n"
    "v_mfma_f32_16x16x32_bf16 a[72:75], v[92:95], v[24:27], a[72:75]\n"
    "ds_read_b64_tr_b16 v[110:111], v122 offset:27648\n"
    "v_mfma_f32_16x16x32_bf16 a[76:79], v[92:95], v[28:31], 0\n"
    "buffer_load_dwordx4 v123, s[40:43], s57 offen offset:1024 nt lds\n"
    "v_mfma_f32_16x16x32_bf16 a[76:79], v[88:91], v[12:15], a[76:79]\n"
    "v_xor_b32 v122, 32, v122\n"
    "s_waitcnt lgkmcnt(4)\n"
    "v_mfma_f32_16x16x32_bf16 a[128:131], v[96:99], v[0:3], 0\n"
    "v_exp_f32 v44, v44\n"
    "v_mfma_f32_16x16x32_bf16 a[128:131], v[100:103], v[16:19], a[128:131]\n"
    "v_exp_f32 v45, v45\n"
    "v_mfma_f32_16x16x32_bf16 a[132:135], v[100:103], v[20:23], 0\n"
    "ds_read_b64_tr_b16 v[80:81], v122 offset:0\n"
    "v_add_f32 v119, v119, v44\n"
    "v_mfma_f32_16x16x32_bf16 a[132:135], v[96:99], v[4:7], a[132:135]\n"
    "ds_read_b64_tr_b16 v[82:83], v122 offset:8192\n"
    "v_add_f32 v119, v119, v45\n"
    "v_mfma_f32_16x16x32_bf16 a[136:139], v[96:99], v[8:11], 0\n"
    "ds_read_b64_tr_b16 v[84:85], v122 offset:16384\n"
    "v_cvt_pk_bf16_f32 v44, v44, v45\n"
    "v_mfma_f32_16x16x32_bf16 a[136:139], v[100:103], v[24:27], a[136:139]\n"
    "ds_read_b64_tr_b16 v[86:87], v122 offset:24576\n"
    "v_mfma_f32_16x16x32_bf16 a[140:143], v[100:103], v[28:31], 0\n"
    "buffer_load_dwordx4 v123, s[40:43], s58 offen offset:2048 nt lds\n"
    "v_mfma_f32_16x16x32_bf16 a[140:143], v[96:99], v[12:15], a[140:143]\n"
    "buffer_load_dwordx4 v123, s[40:43], s59 offen offset:3072 nt lds\n"
    "s_waitcnt lgkmcnt(4)\n"
    "v_mfma_f32_16x16x32_bf16 a[192:195], v[104:107], v[0:3], 0\n"
    "v_exp_f32 v46, v46\n"
    "v_mfma_f32_16x16x32_bf16 a[192:195], v[108:111], v[16:19], a[192:195]\n"
    "v_exp_f32 v47, v47\n"
    "v_mfma_f32_16x16x32_bf16 a[196:199], v[108:111], v[20:23], 0\n"
    "ds_read_b64_tr_b16 v[88:89], v122 offset:1024\n"
    "v_add_f32 v119, v119, v46\n"
    "v_mfma_f32_16x16x32_bf16 a[196:199], v[104:107], v[4:7], a[196:199]\n"
    "ds_read_b64_tr_b16 v[90:91], v122 offset:9216\n"
    "v_add_f32 v119, v119, v47\n"
    "v_mfma_f32_16x16x32_bf16 a[200:203], v[104:107], v[8:11], 0\n"
    "ds_read_b64_tr_b16 v[92:93], v122 offset:17408\n"
    "v_cvt_pk_bf16_f32 v45, v46, v47\n"
    "v_mfma_f32_16x16x32_bf16 a[200:203], v[108:111], v[24:27], a[200:203]\n"
    "ds_read_b64_tr_b16 v[94:95], v122 offset:25600\n"
    "s_add_u32 m0, s61, 0xc000\n"
    "v_mfma_f32_16x16x32_bf16 a[204:207], v[108:111], v[28:31], 0\n"
    "buffer_load_dwordx4 v123, s[44:47], s56 offen offset:0 nt lds\n"
    "v_mfma_f32_16x16x32_bf16 a[204:207], v[104:107], v[12:15], a[204:207]\n"
    "s_waitcnt lgkmcnt(4)\n"
    "v_mfma_f32_16x16x32_bf16 a[16:19], v[80:83], v[0:3], 0\n"
    "v_exp_f32 v64, v64\n"
    "v_mfma_f32_16x16x32_bf16 a[16:19], v[84:87], v[16:19], a[16:19]\n"
    "v_exp_f32 v65, v65\n"
    "v_mfma_f32_16x16x32_bf16 a[20:23], v[84:87], v[20:23], 0\n"
    "ds_read_b64_tr_b16 v[96:97], v122 offset:2048\n"
    "v_add_f32 v116, v116, v64\n"
    "v_mfma_f32_16x16x32_bf16 a[20:23], v[80:83], v[4:7], a[20:23]\n"
    "ds_read_b64_tr_b16 v[98:99], v122 offset:10240\n"
    "v_add_f32 v116, v116, v65\n"
    "v_mfma_f32_16x16x32_bf16 a[24:27], v[80:83], v[8:11], 0\n"
    "ds_read_b64_tr_b16 v[100:101], v122 offset:18432\n"
    "v_cvt_pk_bf16_f32 v34, v64, v65\n"
    "v_mfma_f32_16x16x32_bf16 a[24:27], v[84:87], v[24:27], a[24:27]\n"
    "ds_read_b64_tr_b16 v[102:103], v122 offset:26624\n"
    "v_mfma_f32_16x16x32_bf16 a[28:31], v[84:87], v[28:31], 0\n"
    "buffer_load_dwordx4 v123, s[44:47], s57 offen offset:1024 nt lds\n"
    "v_mfma_f32_16x16x32_bf16 a[28:31], v[80:83], v[12:15], a[28:31]\n"
    "s_waitcnt lgkmcnt(4)\n"
    "v_mfma_f32_16x16x32_bf16 a[80:83], v[88:91], v[0:3], 0\n"
    "v_exp_f32 v66, v66\n"
    "v_mfma_f32_16x16x32_bf16 a[80:83], v[92:95], v[16:19], a[80:83]\n"
    "v_exp_f32 v67, v67\n"
    "v_mfma_f32_16x16x32_bf16 a[84:87], v[92:95], v[20:23], 0\n"
    "ds_read_b64_tr_b16 v[104:105], v122 offset:3072\n"
    "v_add_f32 v116, v116, v66\n"
    "v_mfma_f32_16x16x32_bf16 a[84:87], v[88:91], v[4:7], a[84:87]\n"
    "ds_read_b64_tr_b16 v[106:107], v122 offset:11264\n"
    "v_add_f32 v116, v116, v67\n"
    "v_mfma_f32_16x16x32_bf16 a[88:91], v[88:91], v[8:11], 0\n"
    "ds_read_b64_tr_b16 v[108:109], v122 offset:19456\n"
    "v_cvt_pk_bf16_f32 v35, v66, v67\n"
    "v_mfma_f32_16x16x32_bf16 a[88:91], v[92:95], v[24:27], a[88:91]\n"
    "ds_read_b64_tr_b16 v[110:111], v122 offset:27648\n"
    "v_mfma_f32_16x16x32_bf16 a[92:95], v[92:95], v[28:31], 0\n"
    "buffer_load_dwordx4 v123, s[44:47], s58 offen offset:2048 nt lds\n"
    "v_mfma_f32_16x16x32_bf16 a[92:95], v[88:91], v[12:15], a[92:95]\n"
    "s_waitcnt vmcnt(11) lgkmcnt(4)\n"
    "s_barrier\n"
    "v_mfma_f32_16x16x32_bf16 a[144:147], v[96:99], v[0:3], 0\n"
    "v_exp_f32 v68, v68\n"
    "v_mfma_f32_16x16x32_bf16 a[144:147], v[100:103], v[16:19], a[144:147]\n"
    "v_exp_f32 v69, v69\n"
    "v_mfma_f32_16x16x32_bf16 a[148:151], v[100:103], v[20:23], 0\n"
    "ds_read_b128 v[80:83], v120 offset:49152\n"
    "v_add_f32 v117, v117, v68\n"
    "v_mfma_f32_16x16x32_bf16 a[148:151], v[96:99], v[4:7], a[148:151]\n"
    "ds_read_b128 v[84:87], v121 offset:49152\n"
    "v_add_f32 v117, v117, v69\n"
    "v_mfma_f32_16x16x32_bf16 a[152:155], v[96:99], v[8:11], 0\n"
    "ds_read_b128 v[88:91], v120 offset:50176\n"
    "v_cvt_pk_bf16_f32 v38, v68, v69\n"
    "v_mfma_f32_16x16x32_bf16 a[152:155], v[100:103], v[24:27], a[152:155]\n"
    "ds_read_b128 v[92:95], v121 offset:50176\n"
    "v_mfma_f32_16x16x32_bf16 a[156:159], v[100:103], v[28:31], 0\n"
    "buffer_load_dwordx4 v123, s[44:47], s59 offen offset:3072 nt lds\n"
    "v_mfma_f32_16x16x32_bf16 a[156:159], v[96:99], v[12:15], a[156:159]\n"
    "s_waitcnt lgkmcnt(4)\n"
    "v_mfma_f32_16x16x32_bf16 a[208:211], v[104:107], v[0:3], 0\n"
    "v_exp_f32 v70, v70\n"
    "v_mfma_f32_16x16x32_bf16 a[208:211], v[108:111], v[16:19], a[208:211]\n"
    "v_exp_f32 v71, v71\n"
    "v_mfma_f32_16x16x32_bf16 a[212:215], v[108:111], v[20:23], 0\n"
    "ds_read_b128 v[96:99], v120 offset:51200\n"
    "v_add_f32 v117, v117, v70\n"
    "v_mfma_f32_16x16x32_bf16 a[212:215], v[104:107], v[4:7], a[212:215]\n"
    "ds_read_b128 v[100:103], v121 offset:51200\n"
    "v_add_f32 v117, v117, v71\n"
    "v_mfma_f32_16x16x32_bf16 a[216:219], v[104:107], v[8:11], 0\n"
    "v_cvt_pk_bf16_f32 v39, v70, v71\n"
    "v_add_u32 v123, 0x4000, v123\n"
    "v_mfma_f32_16x16x32_bf16 a[216:219], v[108:111], v[24:27], a[216:219]\n"
    "s_add_u32 m0, s60, 0x4000\n"
    "v_mfma_f32_16x16x32_bf16 a[220:223], v[108:111], v[28:31], 0\n"
    "buffer_load_dwordx4 v123, s[40:43], s56 offen offset:0 nt lds\n"
    "v_mfma_f32_16x16x32_bf16 a[220:223], v[104:107], v[12:15], a[220:223]\n"
    "s_waitcnt lgkmcnt(2)\n"
    "v_mfma_f32_16x16x32_bf16 v[48:51], v[80:83], v[128:131], v[112:115]\n"
    "v_exp_f32 v72, v72\n"
    "v_mfma_f32_16x16x32_bf16 v[48:51], v[84:87], v[132:135], v[48:51]\n"
    "v_exp_f32 v73, v73\n"
    "v_mfma_f32_16x16x32_bf16 v[48:51], v[88:91], v[136:139], v[48:51]\n"
    "ds_read_b128 v[104:107], v120 offset:52224\n"
    "v_add_f32 v118, v118, v72\n"
    "v_mfma_f32_16x16x32_bf16 v[48:51], v[92:95], v[140:143], v[48:51]\n"
    "ds_read_b128 v[108:111], v121 offset:52224\n"
    "v_add_f32 v118, v118, v73\n"
    "v_mfma_f32_16x16x32_bf16 v[52:55], v[92:95], v[172:175], v[112:115]\n"
    "v_cvt_pk_bf16_f32 v42, v72, v73\n"
    "v_mfma_f32_16x16x32_bf16 v[52:55], v[88:91], v[168:171], v[52:55]\n"
    "buffer_load_dwordx4 v123, s[40:43], s57 offen offset:1024 nt lds\n"
    "v_mfma_f32_16x16x32_bf16 v[52:55], v[84:87], v[164:167], v[52:55]\n"
    "v_xor_b32 v122, 64, v122\n"
    "v_mfma_f32_16x16x32_bf16 v[52:55], v[80:83], v[160:163], v[52:55]\n"
    "v_mfma_f32_16x16x32_bf16 v[56:59], v[80:83], v[192:195], v[112:115]\n"
    "v_exp_f32 v74, v74\n"
    "v_mfma_f32_16x16x32_bf16 v[56:59], v[84:87], v[196:199], v[56:59]\n"
    "v_exp_f32 v75, v75\n"
    "v_mfma_f32_16x16x32_bf16 v[56:59], v[88:91], v[200:203], v[56:59]\n"
    "v_add_f32 v118, v118, v74\n"
    "v_mfma_f32_16x16x32_bf16 v[56:59], v[92:95], v[204:207], v[56:59]\n"
    "v_add_f32 v118, v118, v75\n"
    "v_mfma_f32_16x16x32_bf16 v[60:63], v[92:95], v[236:239], v[112:115]\n"
    "v_cvt_pk_bf16_f32 v43, v74, v75\n"
    "v_mfma_f32_16x16x32_bf16 v[60:63], v[88:91], v[232:235], v[60:63]\n"
    "buffer_load_dwordx4 v123, s[40:43], s58 offen offset:2048 nt lds\n"
    "v_mfma_f32_16x16x32_bf16 v[60:63], v[84:87], v[228:231], v[60:63]\n"
    "v_mfma_f32_16x16x32_bf16 v[60:63], v[80:83], v[224:227], v[60:63]\n"
    "s_waitcnt lgkmcnt(0)\n"
    "v_mfma_f32_16x16x32_bf16 v[48:51], v[96:99], v[144:147], v[48:51]\n"
    "v_exp_f32 v76, v76\n"
    "v_mfma_f32_16x16x32_bf16 v[48:51], v[100:103], v[148:151], v[48:51]\n"
    "v_exp_f32 v77, v77\n"
    "v_mfma_f32_16x16x32_bf16 v[48:51], v[104:107], v[152:155], v[48:51]\n"
    "ds_read_b64_tr_b16 v[80:81], v122 offset:0\n"
    "v_add_f32 v119, v119, v76\n"
    "v_mfma_f32_16x16x32_bf16 v[48:51], v[108:111], v[156:159], v[48:51]\n"
    "ds_read_b64_tr_b16 v[82:83], v122 offset:8192\n"
    "v_add_f32 v119, v119, v77\n"
    "v_mfma_f32_16x16x32_bf16 v[52:55], v[108:111], v[188:191], v[52:55]\n"
    "ds_read_b64_tr_b16 v[84:85], v122 offset:16384\n"
    "v_cvt_pk_bf16_f32 v46, v76, v77\n"
    "v_mfma_f32_16x16x32_bf16 v[52:55], v[104:107], v[184:187], v[52:55]\n"
    "ds_read_b64_tr_b16 v[86:87], v122 offset:24576\n"
    "v_mfma_f32_16x16x32_bf16 v[52:55], v[100:103], v[180:183], v[52:55]\n"
    "buffer_load_dwordx4 v123, s[40:43], s59 offen offset:3072 nt lds\n"
    "v_mfma_f32_16x16x32_bf16 v[52:55], v[96:99], v[176:179], v[52:55]\n"
    "v_mfma_f32_16x16x32_bf16 v[56:59], v[96:99], v[208:211], v[56:59]\n"
    "v_exp_f32 v78, v78\n"
    "v_mfma_f32_16x16x32_bf16 v[56:59], v[100:103], v[212:215], v[56:59]\n"
    "v_exp_f32 v79, v79\n"
    "v_mfma_f32_16x16x32_bf16 v[56:59], v[104:107], v[216:219], v[56:59]\n"
    "ds_read_b64_tr_b16 v[88:89], v122 offset:1024\n"
    "v_add_f32 v119, v119, v78\n"
    "v_mfma_f32_16x16x32_bf16 v[56:59], v[108:111], v[220:223], v[56:59]\n"
    "ds_read_b64_tr_b16 v[90:91], v122 offset:9216\n"
    "v_add_f32 v119, v119, v79\n"
    "v_mfma_f32_16x16x32_bf16 v[60:63], v[108:111], v[252:255], v[60:63]\n"
    "ds_read_b64_tr_b16 v[92:93], v122 offset:17408\n"
    "v_cvt_pk_bf16_f32 v47, v78, v79\n"
    "v_mfma_f32_16x16x32_bf16 v[60:63], v[104:107], v[248:251], v[60:63]\n"
    "ds_read_b64_tr_b16 v[94:95], v122 offset:25600\n"
    "s_add_u32 m0, s61, 0x10000\n"
    "v_mfma_f32_16x16x32_bf16 v[60:63], v[100:103], v[244:247], v[60:63]\n"
    "buffer_load_dwordx4 v123, s[44:47], s56 offen offset:0 nt lds\n"
    "v_mfma_f32_16x16x32_bf16 v[60:63], v[96:99], v[240:243], v[60:63]\n"
    "s_waitcnt lgkmcnt(4)\n"
    "v_mfma_f32_16x16x32_bf16 a[48:51], v[80:83], v[0:3], 0\n"
    "v_exp_f32 v48, v48\n"
    "v_mfma_f32_16x16x32_bf16 a[48:51], v[84:87], v[16:19], a[48:51]\n"
    "v_exp_f32 v49, v49\n"
    "v_mfma_f32_16x16x32_bf16 a[52:55], v[84:87], v[20:23], 0\n"
    "ds_read_b64_tr_b16 v[96:97], v122 offset:2048\n"
    "v_add_f32 v116, v116, v48\n"
    "v_mfma_f32_16x16x32_bf16 a[52:55], v[80:83], v[4:7], a[52:55]\n"
    "ds_read_b64_tr_b16 v[98:99], v122 offset:10240\n"
    "v_add_f32 v116, v116, v49\n"
    "v_mfma_f32_16x16x32_bf16 a[56:59], v[80:83], v[8:11], 0\n"
    "ds_read_b64_tr_b16 v[100:101], v122 offset:18432\n"
    "v_cvt_pk_bf16_f32 v48, v48, v49\n"
    "v_mfma_f32_16x16x32_bf16 a[56:59], v[84:87], v[24:27], a[56:59]\n"
    "ds_read_b64_tr_b16 v[102:103], v122 offset:26624\n"
    "v_mfma_f32_16x16x32_bf16 a[60:63], v[84:87], v[28:31], 0\n"
    "buffer_load_dwordx4 v123, s[44:47], s57 offen offset:1024 nt lds\n"
    "v_mfma_f32_16x16x32_bf16 a[60:63], v[80:83], v[12:15], a[60:63]\n"
    "s_waitcnt lgkmcnt(4)\n"
    "v_mfma_f32_16x16x32_bf16 a[112:115], v[88:91], v[0:3], 0\n"
    "v_exp_f32 v50, v50\n"
    "v_mfma_f32_16x16x32_bf16 a[112:115], v[92:95], v[16:19], a[112:115]\n"
    "v_exp_f32 v51, v51\n"
    "v_mfma_f32_16x16x32_bf16 a[116:119], v[92:95], v[20:23], 0\n"
    "ds_read_b64_tr_b16 v[104:105], v122 offset:3072\n"
    "v_add_f32 v116, v116, v50\n"
    "v_mfma_f32_16x16x32_bf16 a[116:119], v[88:91], v[4:7], a[116:119]\n"
    "ds_read_b64_tr_b16 v[106:107], v122 offset:11264\n"
    "v_add_f32 v116, v116, v51\n"
    "v_mfma_f32_16x16x32_bf16 a[120:123], v[88:91], v[8:11], 0\n"
    "ds_read_b64_tr_b16 v[108:109], v122 offset:19456\n"
    "v_cvt_pk_bf16_f32 v49, v50, v51\n"
    "v_mfma_f32_16x16x32_bf16 a[120:123], v[92:95], v[24:27], a[120:123]\n"
    "ds_read_b64_tr_b16 v[110:111], v122 offset:27648\n"
    "v_mfma_f32_16x16x32_bf16 a[124:127], v[92:95], v[28:31], 0\n"
    "buffer_load_dwordx4 v123, s[44:47], s58 offen offset:2048 nt lds\n"
    "v_mfma_f32_16x16x32_bf16 a[124:127], v[88:91], v[12:15], a[124:127]\n"
    "s_waitcnt lgkmcnt(4)\n"
    "v_mfma_f32_16x16x32_bf16 a[176:179], v[96:99], v[0:3], 0\n"
    "v_exp_f32 v52, v52\n"
    "v_mfma_f32_16x16x32_bf16 a[176:179], v[100:103], v[16:19], a[176:179]\n"
    "v_exp_f32 v53, v53\n"
    "v_mfma_f32_16x16x32_bf16 a[180:183], v[100:103], v[20:23], 0\n"
    "ds_read_b128 v[80:83], v120 offset:57344\n"
    "v_add_f32 v117, v117, v52\n"
    "v_mfma_f32_16x16x32_bf16 a[180:183], v[96:99], v[4:7], a[180:183]\n"
    "ds_read_b128 v[84:87], v121 offset:57344\n"
    "v_add_f32 v117, v117, v53\n"
    "v_mfma_f32_16x16x32_bf16 a[184:187], v[96:99], v[8:11], 0\n"
    "ds_read_b128 v[88:91], v120 offset:58368\n"
    "v_cvt_pk_bf16_f32 v52, v52, v53\n"
    "v_mfma_f32_16x16x32_bf16 a[184:187], v[100:103], v[24:27], a[184:187]\n"
    "ds_read_b128 v[92:95], v121 offset:58368\n"
    "v_mfma_f32_16x16x32_bf16 a[188:191], v[100:103], v[28:31], 0\n"
    "buffer_load_dwordx4 v123, s[44:47], s59 offen offset:3072 nt lds\n"
    "v_mfma_f32_16x16x32_bf16 a[188:191], v[96:99], v[12:15], a[188:191]\n"
    "s_waitcnt lgkmcnt(4)\n"
    "v_mfma_f32_16x16x32_bf16 a[240:243], v[104:107], v[0:3], 0\n"
    "v_exp_f32 v54, v54\n"
    "v_mfma_f32_16x16x32_bf16 a[240:243], v[108:111], v[16:19], a[240:243]\n"
    "v_exp_f32 v55, v55\n"
    "v_mfma_f32_16x16x32_bf16 a[244:247], v[108:111], v[20:23], 0\n"
    "ds_read_b128 v[96:99], v120 offset:59392\n"
    "v_add_f32 v117, v117, v54\n"
    "v_mfma_f32_16x16x32_bf16 a[244:247], v[104:107], v[4:7], a[244:247]\n"
    "ds_read_b128 v[100:103], v121 offset:59392\n"
    "v_add_f32 v117, v117, v55\n"
    "v_mfma_f32_16x16x32_bf16 a[248:251], v[104:107], v[8:11], 0\n"
    "v_cvt_pk_bf16_f32 v53, v54, v55\n"
    "v_add_u32 v123, 0x4000, v123\n"
    "v_mfma_f32_16x16x32_bf16 a[248:251], v[108:111], v[24:27], a[248:251]\n"
    "s_add_u32 m0, s60, 0x8000\n"
    "v_mfma_f32_16x16x32_bf16 a[252:255], v[108:111], v[28:31], 0\n"
    "buffer_load_dwordx4 v123, s[40:43], s56 offen offset:0 nt lds\n"
    "v_mfma_f32_16x16x32_bf16 a[252:255], v[104:107], v[12:15], a[252:255]\n"
    "s_waitcnt lgkmcnt(2)\n"
    "v_mfma_f32_16x16x32_bf16 v[64:67], v[80:83], v[128:131], v[112:115]\n"
    "v_exp_f32 v56, v56\n"
    "v_mfma_f32_16x16x32_bf16 v[64:67], v[84:87], v[132:135], v[64:67]\n"
    "v_exp_f32 v57, v57\n"
    "v_mfma_f32_16x16x32_bf16 v[64:67], v[88:91], v[136:139], v[64:67]\n"
    "ds_read_b128 v[104:107], v120 offset:60416\n"
    "v_add_f32 v118, v118, v56\n"
    "v_mfma_f32_16x16x32_bf16 v[64:67], v[92:95], v[140:143], v[64:67]\n"
    "ds_read_b128 v[108:111], v121 offset:60416\n"
    "v_add_f32 v118, v118, v57\n"
    "v_mfma_f32_16x16x32_bf16 v[68:71], v[92:95], v[172:175], v[112:115]\n"
    "v_cvt_pk_bf16_f32 v56, v56, v57\n"
    "v_mfma_f32_16x16x32_bf16 v[68:71], v[88:91], v[168:171], v[68:71]\n"
    "buffer_load_dwordx4 v123, s[40:43], s57 offen offset:1024 nt lds\n"
    "v_mfma_f32_16x16x32_bf16 v[68:71], v[84:87], v[164:167], v[68:71]\n"
    "v_xor_b32 v122, 32, v122\n"
    "v_mfma_f32_16x16x32_bf16 v[68:71], v[80:83], v[160:163], v[68:71]\n"
    "v_mfma_f32_16x16x32_bf16 v[72:75], v[80:83], v[192:195], v[112:115]\n"
    "v_exp_f32 v58, v58\n"
    "v_mfma_f32_16x16x32_bf16 v[72:75], v[84:87], v[196:199], v[72:75]\n"
    "v_exp_f32 v59, v59\n"
    "v_mfma_f32_16x16x32_bf16 v[72:75], v[88:91], v[200:203], v[72:75]\n"
    "v_add_f32 v118, v118, v58\n"
    "v_mfma_f32_16x16x32_bf16 v[72:75], v[92:95], v[204:207], v[72:75]\n"
    "v_add_f32 v118, v118, v59\n"
    "v_mfma_f32_16x16x32_bf16 v[76:79], v[92:95], v[236:239], v[112:115]\n"
    "v_cvt_pk_bf16_f32 v57, v58, v59\n"
    "v_mfma_f32_16x16x32_bf16 v[76:79], v[88:91], v[232:235], v[76:79]\n"
    "buffer_load_dwordx4 v123, s[40:43], s58 offen offset:2048 nt lds\n"
    "v_mfma_f32_16x16x32_bf16 v[76:79], v[84:87], v[228:231], v[76:79]\n"
    "v_mfma_f32_16x16x32_bf16 v[76:79], v[80:83], v[224:227], v[76:79]\n"
    "s_waitcnt lgkmcnt(0)\n"
    "v_mfma_f32_16x16x32_bf16 v[64:67], v[96:99], v[144:147], v[64:67]\n"
    "v_exp_f32 v60, v60\n"
    "v_mfma_f32_16x16x32_bf16 v[64:67], v[100:103], v[148:151], v[64:67]\n"
    "v_exp_f32 v61, v61\n"
    "v_mfma_f32_16x16x32_bf16 v[64:67], v[104:107], v[152:155], v[64:67]\n"
    "ds_read_b64_tr_b16 v[80:81], v122 offset:0\n"
    "v_add_f32 v119, v119, v60\n"
    "v_mfma_f32_16x16x32_bf16 v[64:67], v[108:111], v[156:159], v[64:67]\n"
    "ds_read_b64_tr_b16 v[82:83], v122 offset:8192\n"
    "v_add_f32 v119, v119, v61\n"
    "v_mfma_f32_16x16x32_bf16 v[68:71], v[108:111], v[188:191], v[68:71]\n"
    "ds_read_b64_tr_b16 v[84:85], v122 offset:16384\n"
    "v_cvt_pk_bf16_f32 v60, v60, v61\n"
    "v_mfma_f32_16x16x32_bf16 v[68:71], v[104:107], v[184:187], v[68:71]\n"
    "ds_read_b64_tr_b16 v[86:87], v122 offset:24576\n"
    "v_mfma_f32_16x16x32_bf16 v[68:71], v[100:103], v[180:183], v[68:71]\n"
    "buffer_load_dwordx4 v123, s[40:43], s59 offen offset:3072 nt lds\n"
    "v_mfma_f32_16x16x32_bf16 v[68:71], v[96:99], v[176:179], v[68:71]\n"
    "v_mfma_f32_16x16x32_bf16 v[72:75], v[96:99], v[208:211], v[72:75]\n"
    "v_exp_f32 v62, v62\n"
    "v_mfma_f32_16x16x32_bf16 v[72:75], v[100:103], v[212:215], v[72:75]\n"
    "v_exp_f32 v63, v63\n"
    "v_mfma_f32_16x16x32_bf16 v[72:75], v[104:107], v[216:219], v[72:75]\n"
    "ds_read_b64_tr_b16 v[88:89], v122 offset:1024\n"
    "v_add_f32 v119, v119, v62\n"
    "v_mfma_f32_16x16x32_bf16 v[72:75], v[108:111], v[220:223], v[72:75]\n"
    "ds_read_b64_tr_b16 v[90:91], v122 offset:9216\n"
    "v_add_f32 v119, v119, v63\n"
    "v_mfma_f32_16x16x32_bf16 v[76:79], v[108:111], v[252:255], v[76:79]\n"
    "ds_read_b64_tr_b16 v[92:93], v122 offset:17408\n"
    "v_cvt_pk_bf16_f32 v61, v62, v63\n"
    "v_mfma_f32_16x16x32_bf16 v[76:79], v[104:107], v[248:251], v[76:79]\n"
    "ds_read_b64_tr_b16 v[94:95], v122 offset:25600\n"
    "s_add_u32 m0, s61, 0x14000\n"
    "v_mfma_f32_16x16x32_bf16 v[76:79], v[100:103], v[244:247], v[76:79]\n"
    "buffer_load_dwordx4 v123, s[44:47], s56 offen offset:0 nt lds\n"
    "v_mfma_f32_16x16x32_bf16 v[76:79], v[96:99], v[240:243], v[76:79]\n"
    "s_waitcnt lgkmcnt(4)\n"
    "v_mfma_f32_16x16x32_bf16 a[32:35], v[80:83], v[0:3], 0\n"
    "v_exp_f32 v64, v64\n"
    "v_mfma_f32_16x16x32_bf16 a[32:35], v[84:87], v[16:19], a[32:35]\n"
    "v_exp_f32 v65, v65\n"
    "v_mfma_f32_16x16x32_bf16 a[36:39], v[84:87], v[20:23], 0\n"
    "ds_read_b64_tr_b16 v[96:97], v122 offset:2048\n"
    "v_add_f32 v116, v116, v64\n"
    "v_mfma_f32_16x16x32_bf16 a[36:39], v[80:83], v[4:7], a[36:39]\n"
    "ds_read_b64_tr_b16 v[98:99], v122 offset:10240\n"
    "v_add_f32 v116, v116, v65\n"
    "v_mfma_f32_16x16x32_bf16 a[40:43], v[80:83], v[8:11], 0\n"
    "ds_read_b64_tr_b16 v[100:101], v122 offset:18432\n"
    "v_cvt_pk_bf16_f32 v50, v64, v65\n"
    "v_mfma_f32_16x16x32_bf16 a[40:43], v[84:87], v[24:27], a[40:43]\n"
    "ds_read_b64_tr_b16 v[102:103], v122 offset:26624\n"
    "v_mfma_f32_16x16x32_bf16 a[44:47], v[84:87], v[28:31], 0\n"
    "buffer_load_dwordx4 v123, s[44:47], s57 offen offset:1024 nt lds\n"
    "v_mfma_f32_16x16x32_bf16 a[44:47], v[80:83], v[12:15], a[44:47]\n"
    "s_waitcnt lgkmcnt(4)\n"
    "v_mfma_f32_16x16x32_bf16 a[96:99], v[88:91], v[0:3], 0\n"
    "v_exp_f32 v66, v66\n"
    "v_mfma_f32_16x16x32_bf16 a[96:99], v[92:95], v[16:19], a[96:99]\n"
    "v_exp_f32 v67, v67\n"
    "v_mfma_f32_16x16x32_bf16 a[100:103], v[92:95], v[20:23], 0\n"
    "ds_read_b64_tr_b16 v[104:105], v122 offset:3072\n"
    "v_add_f32 v116, v116, v66\n"
    "v_mfma_f32_16x16x32_bf16 a[100:103], v[88:91], v[4:7], a[100:103]\n"
    "ds_read_b64_tr_b16 v[106:107], v122 offset:11264\n"
    "v_add_f32 v116, v116, v67\n"
    "v_mfma_f32_16x16x32_bf16 a[104:107], v[88:91], v[8:11], 0\n"
    "ds_read_b64_tr_b16 v[108:109], v122 offset:19456\n"
    "v_cvt_pk_bf16_f32 v51, v66, v67\n"
    "v_mfma_f32_16x16x32_bf16 a[104:107], v[92:95], v[24:27], a[104:107]\n"
    "ds_read_b64_tr_b16 v[110:111], v122 offset:27648\n"
    "v_mfma_f32_16x16x32_bf16 a[108:111], v[92:95], v[28:31], 0\n"
    "buffer_load_dwordx4 v123, s[44:47], s58 offen offset:2048 nt lds\n"
    "v_mfma_f32_16x16x32_bf16 a[108:111], v[88:91], v[12:15], a[108:111]\n"
    "s_waitcnt vmcnt(19) lgkmcnt(4)\n"
    "s_barrier\n"
    "v_mfma_f32_16x16x32_bf16 a[160:163], v[96:99], v[0:3], 0\n"
    "v_exp_f32 v68, v68\n"
    "v_mfma_f32_16x16x32_bf16 a[160:163], v[100:103], v[16:19], a[160:163]\n"
    "v_exp_f32 v69, v69\n"
    "v_mfma_f32_16x16x32_bf16 a[164:167], v[100:103], v[20:23], 0\n"
    "ds_read_b128 v[80:83], v120 offset:0\n"
    "v_add_f32 v117, v117, v68\n"
    "v_mfma_f32_16x16x32_bf16 a[164:167], v[96:99], v[4:7], a[164:167]\n"
    "ds_read_b128 v[84:87], v121 offset:0\n"
    "v_add_f32 v117, v117, v69\n"
    "v_mfma_f32_16x16x32_bf16 a[168:171], v[96:99], v[8:11], 0\n"
    "ds_read_b128 v[88:91], v120 offset:1024\n"
    "v_cvt_pk_bf16_f32 v54, v68, v69\n"
    "v_mfma_f32_16x16x32_bf16 a[168:171], v[100:103], v[24:27], a[168:171]\n"
    "ds_read_b128 v[92:95], v121 offset:1024\n"
    "v_mfma_f32_16x16x32_bf16 a[172:175], v[100:103], v[28:31], 0\n"
    "buffer_load_dwordx4 v123, s[44:47], s59 offen offset:3072 nt lds\n"
    "v_mfma_f32_16x16x32_bf16 a[172:175], v[96:99], v[12:15], a[172:175]\n"
    "s_waitcnt lgkmcnt(4)\n"
    "v_mfma_f32_16x16x32_bf16 a[224:227], v[104:107], v[0:3], 0\n"
    "v_exp_f32 v70, v70\n"
    "v_mfma_f32_16x16x32_bf16 a[224:227], v[108:111], v[16:19], a[224:227]\n"
    "v_exp_f32 v71, v71\n"
    "v_mfma_f32_16x16x32_bf16 a[228:231], v[108:111], v[20:23], 0\n"
    "ds_read_b128 v[96:99], v120 offset:2048\n"
    "v_add_f32 v117, v117, v70\n"
    "v_mfma_f32_16x16x32_bf16 a[228:231], v[104:107], v[4:7], a[228:231]\n"
    "ds_read_b128 v[100:103], v121 offset:2048\n"
    "v_add_f32 v117, v117, v71\n"
    "v_mfma_f32_16x16x32_bf16 a[232:235], v[104:107], v[8:11], 0\n"
    "v_cvt_pk_bf16_f32 v55, v70, v71\n"
    "v_mfma_f32_16x16x32_bf16 a[232:235], v[108:111], v[24:27], a[232:235]\n"
    "v_mfma_f32_16x16x32_bf16 a[236:239], v[108:111], v[28:31], 0\n"
    "s_waitcnt vmcnt(12) lgkmcnt(2)\n"
    "v_mfma_f32_16x16x32_bf16 a[236:239], v[104:107], v[12:15], a[236:239]\n"
    "s_barrier\n"
    "LBODY%=:\n"
    "v_mfma_f32_16x16x32_bf16 v[0:3], v[80:83], v[128:131], v[112:115]\n"
    "v_exp_f32 v72, v72\n"
    "v_mfma_f32_16x16x32_bf16 v[0:3], v[84:87], v[132:135], v[0:3]\n"
    "v_exp_f32 v73, v73\n"
    "v_mfma_f32_16x16x32_bf16 v[0:3], v[88:91], v[136:139], v[0:3]\n"
    "ds_read_b128 v[104:107], v120 offset:3072\n"
    "v_add_f32 v118, v118, v72\n"
    "v_mfma_f32_16x16x32_bf16 v[0:3], v[92:95], v[140:143], v[0:3]\n"
    "ds_read_b128 v[108:111], v121 offset:3072\n"
    "v_add_f32 v118, v118, v73\n"
    "v_mfma_f32_16x16x32_bf16 v[4:7], v[92:95], v[172:175], v[112:115]\n"
    "v_cvt_pk_bf16_f32 v58, v72, v73\n"
    "v_add_u32 v123, 0x4000, v123\n"
    "v_mfma_f32_16x16x32_bf16 v[4:7], v[88:91], v[168:171], v[4:7]\n"
    "s_add_u32 m0, s60, 0xc000\n"
    "v_add_u32 v122, s64, v122\n"
    "v_mfma_f32_16x16x32_bf16 v[4:7], v[84:87], v[164:167], v[4:7]\n"
    "buffer_load_dwordx4 v123, s[40:43], s56 offen offset:0 nt lds\n"
    "v_mfma_f32_16x16x32_bf16 v[4:7], v[80:83], v[160:163], v[4:7]\n"
    "v_xor_b32 v122, 64, v122\n"
    "v_mfma_f32_16x16x32_bf16 v[8:11], v[80:83], v[192:195], v[112:115]\n"
    "v_exp_f32 v74, v74\n"
    "v_mfma_f32_16x16x32_bf16 v[8:11], v[84:87], v[196:199], v[8:11]\n"
    "v_exp_f32 v75, v75\n"
    "v_mfma_f32_16x16x32_bf16 v[8:11], v[88:91], v[200:203], v[8:11]\n"
    "v_add_f32 v118, v118, v74\n"
    "v_mfma_f32_16x16x32_bf16 v[8:11], v[92:95], v[204:207], v[8:11]\n"
    "v_add_f32 v118, v118, v75\n"
    "v_mfma_f32_16x16x32_bf16 v[12:15], v[92:95], v[236:239], v[112:115]\n"
    "v_cvt_pk_bf16_f32 v59, v74, v75\n"
    "v_mfma_f32_16x16x32_bf16 v[12:15], v[88:91], v[232:235], v[12:15]\n"
    "v_mfma_f32_16x16x32_bf16 v[12:15], v[84:87], v[228:231], v[12:15]\n"
    "v_mfma_f32_16x16x32_bf16 v[12:15], v[80:83], v[224:227], v[12:15]\n"
    "s_waitcnt lgkmcnt(0)\n"
    "v_mfma_f32_16x16x32_bf16 v[0:3], v[96:99], v[144:147], v[0:3]\n"
    "v_exp_f32 v76, v76\n"
    "v_mfma_f32_16x16x32_bf16 v[0:3], v[100:103], v[148:151], v[0:3]\n"
    "v_exp_f32 v77, v77\n"
    "v_mfma_f32_16x16x32_bf16 v[0:3], v[104:107], v[152:155], v[0:3]\n"
    "ds_read_b64_tr_b16 v[80:81], v122 offset:0\n"
    "v_add_f32 v119, v119, v76\n"
    "v_mfma_f32_16x16x32_bf16 v[0:3], v[108:111], v[156:159], v[0:3]\n"
    "ds_read_b64_tr_b16 v[82:83], v122 offset:8192\n"
    "v_add_f32 v119, v119, v77\n"
    "v_mfma_f32_16x16x32_bf16 v[4:7], v[108:111], v[188:191], v[4:7]\n"
    "ds_read_b64_tr_b16 v[84:85], v122 offset:16384\n"
    "v_cvt_pk_bf16_f32 v62, v76, v77\n"
    "v_mfma_f32_16x16x32_bf16 v[4:7], v[104:107], v[184:187], v[4:7]\n"
    "ds_read_b64_tr_b16 v[86:87], v122 offset:24576\n"
    "v_mfma_f32_16x16x32_bf16 v[4:7], v[100:103], v[180:183], v[4:7]\n"
    "buffer_load_dwordx4 v123, s[40:43], s57 offen offset:1024 nt lds\n"
    "v_mfma_f32_16x16x32_bf16 v[4:7], v[96:99], v[176:179], v[4:7]\n"
    "v_mfma_f32_16x16x32_bf16 v[8:11], v[96:99], v[208:211], v[8:11]\n"
    "v_exp_f32 v78, v78\n"
    "v_mfma_f32_16x16x32_bf16 v[8:11], v[100:103], v[212:215], v[8:11]\n"
    "v_exp_f32 v79, v79\n"
    "v_mfma_f32_16x16x32_bf16 v[8:11], v[104:107], v[216:219], v[8:11]\n"
    "ds_read_b64_tr_b16 v[88:89], v122 offset:1024\n"
    "v_add_f32 v119, v119, v78\n"
    "v_mfma_f32_16x16x32_bf16 v[8:11], v[108:111], v[220:223], v[8:11]\n"
    "ds_read_b64_tr_b16 v[90:91], v122 offset:9216\n"
    "v_add_f32 v119, v119, v79\n"
    "v_mfma_f32_16x16x32_bf16 v[12:15], v[108:111], v[252:255], v[12:15]\n"
    "ds_read_b64_tr_b16 v[92:93], v122 offset:17408\n"
    "v_cvt_pk_bf16_f32 v63, v78, v79\n"
    "v_mfma_f32_16x16x32_bf16 v[12:15], v[104:107], v[248:251], v[12:15]\n"
    "ds_read_b64_tr_b16 v[94:95], v122 offset:25600\n"
    "v_mfma_f32_16x16x32_bf16 v[12:15], v[100:103], v[244:247], v[12:15]\n"
    "buffer_load_dwordx4 v123, s[40:43], s58 offen offset:2048 nt lds\n"
    "v_mfma_f32_16x16x32_bf16 v[12:15], v[96:99], v[240:243], v[12:15]\n"
    "s_waitcnt lgkmcnt(4)\n"
    "v_mfma_f32_16x16x32_bf16 a[0:3], v[80:83], v[32:35], a[0:3]\n"
    "v_exp_f32 v0, v0\n"
    "v_mfma_f32_16x16x32_bf16 a[0:3], v[84:87], v[48:51], a[0:3]\n"
    "v_exp_f32 v1, v1\n"
    "v_mfma_f32_16x16x32_bf16 a[4:7], v[84:87], v[52:55], a[4:7]\n"
    "ds_read_b64_tr_b16 v[96:97], v122 offset:2048\n"
    "v_add_f32 v116, v116, v0\n"
    "v_mfma_f32_16x16x32_bf16 a[4:7], v[80:83], v[36:39], a[4:7]\n"
    "ds_read_b64_tr_b16 v[98:99], v122 offset:10240\n"
    "v_add_f32 v116, v116, v1\n"
    "v_mfma_f32_16x16x32_bf16 a[8:11], v[80:83], v[40:43], a[8:11]\n"
    "ds_read_b64_tr_b16 v[100:101], v122 offset:18432\n"
    "v_cvt_pk_bf16_f32 v0, v0, v1\n"
    "v_mfma_f32_16x16x32_bf16 a[8:11], v[84:87], v[56:59], a[8:11]\n"
    "ds_read_b64_tr_b16 v[102:103], v122 offset:26624\n"
    "v_mfma_f32_16x16x32_bf16 a[12:15], v[84:87], v[60:63], a[12:15]\n"
    "v_mfma_f32_16x16x32_bf16 a[12:15], v[80:83], v[44:47], a[12:15]\n"
    "s_waitcnt lgkmcnt(4)\n"
    "v_mfma_f32_16x16x32_bf16 a[64:67], v[88:91], v[32:35], a[64:67]\n"
    "v_exp_f32 v2, v2\n"
    "v_mfma_f32_16x16x32_bf16 a[64:67], v[92:95], v[48:51], a[64:67]\n"
    "v_exp_f32 v3, v3\n"
    "v_mfma_f32_16x16x32_bf16 a[68:71], v[92:95], v[52:55], a[68:71]\n"
    "ds_read_b64_tr_b16 v[104:105], v122 offset:3072\n"
    "v_add_f32 v116, v116, v2\n"
    "v_mfma_f32_16x16x32_bf16 a[68:71], v[88:91], v[36:39], a[68:71]\n"
    "ds_read_b64_tr_b16 v[106:107], v122 offset:11264\n"
    "v_add_f32 v116, v116, v3\n"
    "v_mfma_f32_16x16x32_bf16 a[72:75], v[88:91], v[40:43], a[72:75]\n"
    "ds_read_b64_tr_b16 v[108:109], v122 offset:19456\n"
    "v_cvt_pk_bf16_f32 v1, v2, v3\n"
    "v_mfma_f32_16x16x32_bf16 a[72:75], v[92:95], v[56:59], a[72:75]\n"
    "ds_read_b64_tr_b16 v[110:111], v122 offset:27648\n"
    "v_mfma_f32_16x16x32_bf16 a[76:79], v[92:95], v[60:63], a[76:79]\n"
    "buffer_load_dwordx4 v123, s[40:43], s59 offen offset:3072 nt lds\n"
    "v_mfma_f32_16x16x32_bf16 a[76:79], v[88:91], v[44:47], a[76:79]\n"
    "s_waitcnt lgkmcnt(4)\n"
    "v_mfma_f32_16x16x32_bf16 a[128:131], v[96:99], v[32:35], a[128:131]\n"
    "v_exp_f32 v4, v4\n"
    "v_mfma_f32_16x16x32_bf16 a[128:131], v[100:103], v[48:51], a[128:131]\n"
    "v_exp_f32 v5, v5\n"
    "v_mfma_f32_16x16x32_bf16 a[132:135], v[100:103], v[52:55], a[132:135]\n"
    "ds_read_b128 v[80:83], v120 offset:8192\n"
    "v_add_f32 v117, v117, v4\n"
    "v_mfma_f32_16x16x32_bf16 a[132:135], v[96:99], v[36:39], a[132:135]\n"
    "ds_read_b128 v[84:87], v121 offset:8192\n"
    "v_add_f32 v117, v117, v5\n"
    "v_mfma_f32_16x16x32_bf16 a[136:139], v[96:99], v[40:43], a[136:139]\n"
    "ds_read_b128 v[88:91], v120 offset:9216\n"
    "v_cvt_pk_bf16_f32 v4, v4, v5\n"
    "v_mfma_f32_16x16x32_bf16 a[136:139], v[100:103], v[56:59], a[136:139]\n"
    "ds_read_b128 v[92:95], v121 offset:9216\n"
    "v_mfma_f32_16x16x32_bf16 a[140:143], v[100:103], v[60:63], a[140:143]\n"
    "v_mfma_f32_16x16x32_bf16 a[140:143], v[96:99], v[44:47], a[140:143]\n"
    "s_waitcnt lgkmcnt(4)\n"
    "v_mfma_f32_16x16x32_bf16 a[192:195], v[104:107], v[32:35], a[192:195]\n"
    "v_exp_f32 v6, v6\n"
    "v_mfma_f32_16x16x32_bf16 a[192:195], v[108:111], v[48:51], a[192:195]\n"
    "v_exp_f32 v7, v7\n"
    "v_mfma_f32_16x16x32_bf16 a[196:199], v[108:111], v[52:55], a[196:199]\n"
    "ds_read_b128 v[96:99], v120 offset:10240\n"
    "v_add_f32 v117, v117, v6\n"
    "v_mfma_f32_16x16x32_bf16 a[196:199], v[104:107], v[36:39], a[196:199]\n"
    "ds_read_b128 v[100:103], v121 offset:10240\n"
    "v_add_f32 v117, v117, v7\n"
    "v_mfma_f32_16x16x32_bf16 a[200:203], v[104:107], v[40:43], a[200:203]\n"
    "v_cvt_pk_bf16_f32 v5, v6, v7\n"
    "v_mfma_f32_16x16x32_bf16 a[200:203], v[108:111], v[56:59], a[200:203]\n"
    "v_mfma_f32_16x16x32_bf16 a[204:207], v[108:111], v[60:63], a[204:207]\n"
    "v_mfma_f32_16x16x32_bf16 a[204:207], v[104:107], v[44:47], a[204:207]\n"
    "s_waitcnt lgkmcnt(2)\n"
    "v_mfma_f32_16x16x32_bf16 v[64:67], v[80:83], v[128:131], v[112:115]\n"
    "v_exp_f32 v8, v8\n"
    "v_mfma_f32_16x16x32_bf16 v[64:67], v[84:87], v[132:135], v[64:67]\n"
    "v_exp_f32 v9, v9\n"
    "v_mfma_f32_16x16x32_bf16 v[64:67], v[88:91], v[136:139], v[64:67]\n"
    "ds_read_b128 v[104:107], v120 offset:11264\n"
    "v_add_f32 v118, v118, v8\n"
    "v_mfma_f32_16x16x32_bf16 v[64:67], v[92:95], v[140:143], v[64:67]\n"
    "ds_read_b128 v[108:111], v121 offset:11264\n"
    "v_add_f32 v118, v118, v9\n"
    "v_mfma_f32_16x16x32_bf16 v[68:71], v[92:95], v[172:175], v[112:115]\n"
    "v_cvt_pk_bf16_f32 v8, v8, v9\n"
    "s_add_u32 m0, s62, 0x0\n"
    "v_mfma_f32_16x16x32_bf16 v[68:71], v[88:91], v[168:171], v[68:71]\n"
    "buffer_load_dwordx4 v123, s[44:47], s56 offen offset:0 nt lds\n"
    "v_mfma_f32_16x16x32_bf16 v[68:71], v[84:87], v[164:167], v[68:71]\n"
    "v_xor_b32 v122, 32, v122\n"
    "v_mfma_f32_16x16x32_bf16 v[68:71], v[80:83], v[160:163], v[68:71]\n"
    "v_mfma_f32_16x16x32_bf16 v[72:75], v[80:83], v[192:195], v[112:115]\n"
    "v_exp_f32 v10, v10\n"
    "v_mfma_f32_16x16x32_bf16 v[72:75], v[84:87], v[196:199], v[72:75]\n"
    "v_exp_f32 v11, v11\n"
    "v_mfma_f32_16x16x32_bf16 v[72:75], v[88:91], v[200:203], v[72:75]\n"
    "v_add_f32 v118, v118, v10\n"
    "v_mfma_f32_16x16x32_bf16 v[72:75], v[92:95], v[204:207], v[72:75]\n"
    "v_add_f32 v118, v118, v11\n"
    "v_mfma_f32_16x16x32_bf16 v[76:79], v[92:95], v[236:239], v[112:115]\n"
    "v_cvt_pk_bf16_f32 v9, v10, v11\n"
    "v_mfma_f32_16x16x32_bf16 v[76:79], v[88:91], v[232:235], v[76:79]\n"
    "v_mfma_f32_16x16x32_bf16 v[76:79], v[84:87], v[228:231], v[76:79]\n"
    "v_mfma_f32_16x16x32_bf16 v[76:79], v[80:83], v[224:227], v[76:79]\n"
    "s_waitcnt lgkmcnt(0)\n"
    "v_mfma_f32_16x16x32_bf16 v[64:67], v[96:99], v[144:147], v[64:67]\n"
    "v_exp_f32 v12, v12\n"
    "v_mfma_f32_16x16x32_bf16 v[64:67], v[100:103], v[148:151], v[64:67]\n"
    "v_exp_f32 v13, v13\n"
    "v_mfma_f32_16x16x32_bf16 v[64:67], v[104:107], v[152:155], v[64:67]\n"
    "ds_read_b64_tr_b16 v[80:81], v122 offset:0\n"
    "v_add_f32 v119, v119, v12\n"
    "v_mfma_f32_16x16x32_bf16 v[64:67], v[108:111], v[156:159], v[64:67]\n"
    "ds_read_b64_tr_b16 v[82:83], v122 offset:8192\n"
    "v_add_f32 v119, v119, v13\n"
    "v_mfma_f32_16x16x32_bf16 v[68:71], v[108:111], v[188:191], v[68:71]\n"
    "ds_read_b64_tr_b16 v[84:85], v122 offset:16384\n"
    "v_cvt_pk_bf16_f32 v12, v12, v13\n"
    "v_mfma_f32_16x16x32_bf16 v[68:71], v[104:107], v[184:187], v[68:71]\n"
    "ds_read_b64_tr_b16 v[86:87], v122 offset:24576\n"
    "v_mfma_f32_16x16x32_bf16 v[68:71], v[100:103], v[180:183], v[68:71]\n"
    "buffer_load_dwordx4 v123, s[44:47], s57 offen offset:1024 nt lds\n"
    "v_mfma_f32_16x16x32_bf16 v[68:71], v[96:99], v[176:179], v[68:71]\n"
    "v_mfma_f32_16x16x32_bf16 v[72:75], v[96:99], v[208:211], v[72:75]\n"
    "v_exp_f32 v14, v14\n"
    "v_mfma_f32_16x16x32_bf16 v[72:75], v[100:103], v[212:215], v[72:75]\n"
    "v_exp_f32 v15, v15\n"
    "v_mfma_f32_16x16x32_bf16 v[72:75], v[104:107], v[216:219], v[72:75]\n"
    "ds_read_b64_tr_b16 v[88:89], v122 offset:1024\n"
    "v_add_f32 v119, v119, v14\n"
    "v_mfma_f32_16x16x32_bf16 v[72:75], v[108:111], v[220:223], v[72:75]\n"
    "ds_read_b64_tr_b16 v[90:91], v122 offset:9216\n"
    "v_add_f32 v119, v119, v15\n"
    "v_mfma_f32_16x16x32_bf16 v[76:79], v[108:111], v[252:255], v[76:79]\n"
    "ds_read_b64_tr_b16 v[92:93], v122 offset:17408\n"
    "v_cvt_pk_bf16_f32 v13, v14, v15\n"
    "v_mfma_f32_16x16x32_bf16 v[76:79], v[104:107], v[248:251], v[76:79]\n"
    "ds_read_b64_tr_b16 v[94:95], v122 offset:25600\n"
    "v_mfma_f32_16x16x32_bf16 v[76:79], v[100:103], v[244:247], v[76:79]\n"
    "buffer_load_dwordx4 v123, s[44:47], s58 offen offset:2048 nt lds\n"
    "v_mfma_f32_16x16x32_bf16 v[76:79], v[96:99], v[240:243], v[76:79]\n"
    "s_waitcnt lgkmcnt(4)\n"
    "v_mfma_f32_16x16x32_bf16 a[16:19], v[80:83], v[32:35], a[16:19]\n"
    "v_exp_f32 v64, v64\n"
    "v_mfma_f32_16x16x32_bf16 a[16:19], v[84:87], v[48:51], a[16:19]\n"
    "v_exp_f32 v65, v65\n"
    "v_mfma_f32_16x16x32_bf16 a[20:23], v[84:87], v[52:55], a[20:23]\n"
    "ds_read_b64_tr_b16 v[96:97], v122 offset:2048\n"
    "v_add_f32 v116, v116, v64\n"
    "v_mfma_f32_16x16x32_bf16 a[20:23], v[80:83], v[36:39], a[20:23]\n"
    "ds_read_b64_tr_b16 v[98:99], v122 offset:10240\n"
    "v_add_f32 v116, v116, v65\n"
    "v_mfma_f32_16x16x32_bf16 a[24:27], v[80:83], v[40:43], a[24:27]\n"
    "ds_read_b64_tr_b16 v[100:101], v122 offset:18432\n"
    "v_cvt_pk_bf16_f32 v2, v64, v65\n"
    "v_mfma_f32_16x16x32_bf16 a[24:27], v[84:87], v[56:59], a[24:27]\n"
    "ds_read_b64_tr_b16 v[102:103], v122 offset:26624\n"
    "v_mfma_f32_16x16x32_bf16 a[28:31], v[84:87], v[60:63], a[28:31]\n"
    "v_mfma_f32_16x16x32_bf16 a[28:31], v[80:83], v[44:47], a[28:31]\n"
    "s_waitcnt lgkmcnt(4)\n"
    "v_mfma_f32_16x16x32_bf16 a[80:83], v[88:91], v[32:35], a[80:83]\n"
    "v_exp_f32 v66, v66\n"
    "v_mfma_f32_16x16x32_bf16 a[80:83], v[92:95], v[48:51], a[80:83]\n"
    "v_exp_f32 v67, v67\n"
    "v_mfma_f32_16x16x32_bf16 a[84:87], v[92:95], v[52:55], a[84:87]\n"
    "ds_read_b64_tr_b16 v[104:105], v122 offset:3072\n"
    "v_add_f32 v116, v116, v66\n"
    "v_mfma_f32_16x16x32_bf16 a[84:87], v[88:91], v[36:39], a[84:87]\n"
    "ds_read_b64_tr_b16 v[106:107], v122 offset:11264\n"
    "v_add_f32 v116, v116, v67\n"
    "v_mfma_f32_16x16x32_bf16 a[88:91], v[88:91], v[40:43], a[88:91]\n"
    "ds_read_b64_tr_b16 v[108:109], v122 offset:19456\n"
    "v_cvt_pk_bf16_f32 v3, v66, v67\n"
    "v_mfma_f32_16x16x32_bf16 a[88:91], v[92:95], v[56:59], a[88:91]\n"
    "ds_read_b64_tr_b16 v[110:111], v122 offset:27648\n"
    "v_mfma_f32_16x16x32_bf16 a[92:95], v[92:95], v[60:63], a[92:95]\n"
    "buffer_load_dwordx4 v123, s[44:47], s59 offen offset:3072 nt lds\n"
    "v_mfma_f32_16x16x32_bf16 a[92:95], v[88:91], v[44:47], a[92:95]\n"
    "s_waitcnt lgkmcnt(4)\n"
    "v_mfma_f32_16x16x32_bf16 a[144:147], v[96:99], v[32:35], a[144:147]\n"
    "v_exp_f32 v68, v68\n"
    "v_mfma_f32_16x16x32_bf16 a[144:147], v[100:103], v[48:51], a[144:147]\n"
    "v_exp_f32 v69, v69\n"
    "v_mfma_f32_16x16x32_bf16 a[148:151], v[100:103], v[52:55], a[148:151]\n"
    "ds_read_b128 v[80:83], v120 offset:16384\n"
    "v_add_f32 v117, v117, v68\n"
    "v_mfma_f32_16x16x32_bf16 a[148:151], v[96:99], v[36:39], a[148:151]\n"
    "ds_read_b128 v[84:87], v121 offset:16384\n"
    "v_add_f32 v117, v117, v69\n"
    "v_mfma_f32_16x16x32_bf16 a[152:155], v[96:99], v[40:43], a[152:155]\n"
    "ds_read_b128 v[88:91], v120 offset:17408\n"
    "v_cvt_pk_bf16_f32 v6, v68, v69\n"
    "v_mfma_f32_16x16x32_bf16 a[152:155], v[100:103], v[56:59], a[152:155]\n"
    "ds_read_b128 v[92:95], v121 offset:17408\n"
    "v_mfma_f32_16x16x32_bf16 a[156:159], v[100:103], v[60:63], a[156:159]\n"
    "v_mfma_f32_16x16x32_bf16 a[156:159], v[96:99], v[44:47], a[156:159]\n"
    "s_waitcnt lgkmcnt(4)\n"
    "v_mfma_f32_16x16x32_bf16 a[208:211], v[104:107], v[32:35], a[208:211]\n"
    "v_exp_f32 v70, v70\n"
    "v_mfma_f32_16x16x32_bf16 a[208:211], v[108:111], v[48:51], a[208:211]\n"
    "v_exp_f32 v71, v71\n"
    "v_mfma_f32_16x16x32_bf16 a[212:215], v[108:111], v[52:55], a[212:215]\n"
    "ds_read_b128 v[96:99], v120 offset:18432\n"
    "v_add_f32 v117, v117, v70\n"
    "v_mfma_f32_16x16x32_bf16 a[212:215], v[104:107], v[36:39], a[212:215]\n"
    "ds_read_b128 v[100:103], v121 offset:18432\n"
    "v_add_f32 v117, v117, v71\n"
    "v_mfma_f32_16x16x32_bf16 a[216:219], v[104:107], v[40:43], a[216:219]\n"
    "v_cvt_pk_bf16_f32 v7, v70, v71\n"
    "v_mfma_f32_16x16x32_bf16 a[216:219], v[108:111], v[56:59], a[216:219]\n"
    "v_mfma_f32_16x16x32_bf16 a[220:223], v[108:111], v[60:63], a[220:223]\n"
    "s_waitcnt vmcnt(12) lgkmcnt(2)\n"
    "v_mfma_f32_16x16x32_bf16 a[220:223], v[104:107], v[44:47], a[220:223]\n"
    "s_barrier\n"
    "v_mfma_f32_16x16x32_bf16 v[16:19], v[80:83], v[128:131], v[112:115]\n"
    "v_exp_f32 v72, v72\n"
    "v_mfma_f32_16x16x32_bf16 v[16:19], v[84:87], v[132:135], v[16:19]\n"
    "v_exp_f32 v73, v73\n"
    "v_mfma_f32_16x16x32_bf16 v[16:19], v[88:91], v[136:139], v[16:19]\n"
    "ds_read_b128 v[104:107], v120 offset:19456\n"
    "v_add_f32 v118, v118, v72\n"
    "v_mfma_f32_16x16x32_bf16 v[16:19], v[92:95], v[140:143], v[16:19]\n"
    "ds_read_b128 v[108:111], v121 offset:19456\n"
    "v_add_f32 v118, v118, v73\n"
    "v_mfma_f32_16x16x32_bf16 v[20:23], v[92:95], v[172:175], v[112:115]\n"
    "v_cvt_pk_bf16_f32 v10, v72, v73\n"
    "v_add_u32 v123, 0x4000, v123\n"
    "v_mfma_f32_16x16x32_bf16 v[20:23], v[88:91], v[168:171], v[20:23]\n"
    "s_add_u32 m0, s60, 0x0\n"
    "v_xor_b32 v122, 64, v122\n"
    "v_mfma_f32_16x16x32_bf16 v[20:23], v[84:87], v[164:167], v[20:23]\n"
    "buffer_load_dwordx4 v123, s[40:43], s56 offen offset:0 nt lds\n"
    "v_mfma_f32_16x16x32_bf16 v[20:23], v[80:83], v[160:163], v[20:23]\n"
    "v_mfma_f32_16x16x32_bf16 v[24:27], v[80:83], v[192:195], v[112:115]\n"
    "v_exp_f32 v74, v74\n"
    "v_mfma_f32_16x16x32_bf16 v[24:27], v[84:87], v[196:199], v[24:27]\n"
    "v_exp_f32 v75, v75\n"
    "v_mfma_f32_16x16x32_bf16 v[24:27], v[88:91], v[200:203], v[24:27]\n"
    "v_add_f32 v118, v118, v74\n"
    "v_mfma_f32_16x16x32_bf16 v[24:27], v[92:95], v[204:207], v[24:27]\n"
    "v_add_f32 v118, v118, v75\n"
    "v_mfma_f32_16x16x32_bf16 v[28:31], v[92:95], v[236:239], v[112:115]\n"
    "v_cvt_pk_bf16_f32 v11, v74, v75\n"
    "v_mfma_f32_16x16x32_bf16 v[28:31], v[88:91], v[232:235], v[28:31]\n"
    "v_mfma_f32_16x16x32_bf16 v[28:31], v[84:87], v[228:231], v[28:31]\n"
    "v_mfma_f32_16x16x32_bf16 v[28:31], v[80:83], v[224:227], v[28:31]\n"
    "s_waitcnt lgkmcnt(0)\n"
    "v_mfma_f32_16x16x32_bf16 v[16:19], v[96:99], v[144:147], v[16:19]\n"
    "v_exp_f32 v76, v76\n"
    "v_mfma_f32_16x16x32_bf16 v[16:19], v[100:103], v[148:151], v[16:19]\n"
    "v_exp_f32 v77, v77\n"
    "v_mfma_f32_16x16x32_bf16 v[16:19], v[104:107], v[152:155], v[16:19]\n"
    "ds_read_b64_tr_b16 v[80:81], v122 offset:0\n"
    "v_add_f32 v119, v119, v76\n"
    "v_mfma_f32_16x16x32_bf16 v[16:19], v[108:111], v[156:159], v[16:19]\n"
    "ds_read_b64_tr_b16 v[82:83], v122 offset:8192\n"
    "v_add_f32 v119, v119, v77\n"
    "v_mfma_f32_16x16x32_bf16 v[20:23], v[108:111], v[188:191], v[20:23]\n"
    "ds_read_b64_tr_b16 v[84:85], v122 offset:16384\n"
    "v_cvt_pk_bf16_f32 v14, v76, v77\n"
    "v_mfma_f32_16x16x32_bf16 v[20:23], v[104:107], v[184:187], v[20:23]\n"
    "ds_read_b64_tr_b16 v[86:87], v122 offset:24576\n"
    "v_mfma_f32_16x16x32_bf16 v[20:23], v[100:103], v[180:183], v[20:23]\n"
    "buffer_load_dwordx4 v123, s[40:43], s57 offen offset:1024 nt lds\n"
    "v_mfma_f32_16x16x32_bf16 v[20:23], v[96:99], v[176:179], v[20:23]\n"
    "global_load_dword v127, v126, s[38:39]\n"
    "v_mfma_f32_16x16x32_bf16 v[24:27], v[96:99], v[208:211], v[24:27]\n"
    "v_exp_f32 v78, v78\n"
    "v_mfma_f32_16x16x32_bf16 v[24:27], v[100:103], v[212:215], v[24:27]\n"
    "v_exp_f32 v79, v79\n"
    "v_mfma_f32_16x16x32_bf16 v[24:27], v[104:107], v[216:219], v[24:27]\n"
    "ds_read_b64_tr_b16 v[88:89], v122 offset:1024\n"
    "v_add_f32 v119, v119, v78\n"
    "v_mfma_f32_16x16x32_bf16 v[24:27], v[108:111], v[220:223], v[24:27]\n"
    "ds_read_b64_tr_b16 v[90:91], v122 offset:9216\n"
    "v_add_f32 v119, v119, v79\n"
    "v_mfma_f32_16x16x32_bf16 v[28:31], v[108:111], v[252:255], v[28:31]\n"
    "ds_read_b64_tr_b16 v[92:93], v122 offset:17408\n"
    "v_cvt_pk_bf16_f32 v15, v78, v79\n"
    "v_mfma_f32_16x16x32_bf16 v[28:31], v[104:107], v[248:251], v[28:31]\n"
    "ds_read_b64_tr_b16 v[94:95], v122 offset:25600\n"
    "v_mfma_f32_16x16x32_bf16 v[28:31], v[100:103], v[244:247], v[28:31]\n"
    "buffer_load_dwordx4 v123, s[40:43], s58 offen offset:2048 nt lds\n"
    "v_mfma_f32_16x16x32_bf16 v[28:31], v[96:99], v[240:243], v[28:31]\n"
    "s_waitcnt lgkmcnt(4)\n"
    "v_mfma_f32_16x16x32_bf16 a[48:51], v[80:83], v[32:35], a[48:51]\n"
    "v_exp_f32 v16, v16\n"
    "v_mfma_f32_16x16x32_bf16 a[48:51], v[84:87], v[48:51], a[48:51]\n"
    "v_exp_f32 v17, v17\n"
    "v_mfma_f32_16x16x32_bf16 a[52:55], v[84:87], v[52:55], a[52:55]\n"
    "ds_read_b64_tr_b16 v[96:97], v122 offset:2048\n"
    "v_add_f32 v116, v116, v16\n"
    "v_mfma_f32_16x16x32_bf16 a[52:55], v[80:83], v[36:39], a[52:55]\n"
    "ds_read_b64_tr_b16 v[98:99], v122 offset:10240\n"
    "v_add_f32 v116, v116, v17\n"
    "v_mfma_f32_16x16x32_bf16 a[56:59], v[80:83], v[40:43], a[56:59]\n"
    "ds_read_b64_tr_b16 v[100:101], v122 offset:18432\n"
    "v_cvt_pk_bf16_f32 v16, v16, v17\n"
    "v_mfma_f32_16x16x32_bf16 a[56:59], v[84:87], v[56:59], a[56:59]\n"
    "ds_read_b64_tr_b16 v[102:103], v122 offset:26624\n"
    "v_mfma_f32_16x16x32_bf16 a[60:63], v[84:87], v[60:63], a[60:63]\n"
    "v_mfma_f32_16x16x32_bf16 a[60:63], v[80:83], v[44:47], a[60:63]\n"
    "s_waitcnt lgkmcnt(4)\n"
    "v_mfma_f32_16x16x32_bf16 a[112:115], v[88:91], v[32:35], a[112:115]\n"
    "v_exp_f32 v18, v18\n"
    "v_mfma_f32_16x16x32_bf16 a[112:115], v[92:95], v[48:51], a[112:115]\n"
    "v_exp_f32 v19, v19\n"
    "v_mfma_f32_16x16x32_bf16 a[116:119], v[92:95], v[52:55], a[116:119]\n"
    "ds_read_b64_tr_b16 v[104:105], v122 offset:3072\n"
    "v_add_f32 v116, v116, v18\n"
    "v_mfma_f32_16x16x32_bf16 a[116:119], v[88:91], v[36:39], a[116:119]\n"
    "ds_read_b64_tr_b16 v[106:107], v122 offset:11264\n"
    "v_add_f32 v116, v116, v19\n"
    "v_mfma_f32_16x16x32_bf16 a[120:123], v[88:91], v[40:43], a[120:123]\n"
    "ds_read_b64_tr_b16 v[108:109], v122 offset:19456\n"
    "v_cvt_pk_bf16_f32 v17, v18, v19\n"
    "v_mfma_f32_16x16x32_bf16 a[120:123], v[92:95], v[56:59], a[120:123]\n"
    "ds_read_b64_tr_b16 v[110:111], v122 offset:27648\n"
    "v_mfma_f32_16x16x32_bf16 a[124:127], v[92:95], v[60:63], a[124:127]\n"
    "buffer_load_dwordx4 v123, s[40:43], s59 offen offset:3072 nt lds\n"
    "v_mfma_f32_16x16x32_bf16 a[124:127], v[88:91], v[44:47], a[124:127]\n"
    "s_waitcnt lgkmcnt(4)\n"
    "v_mfma_f32_16x16x32_bf16 a[176:179], v[96:99], v[32:35], a[176:179]\n"
    "v_exp_f32 v20, v20\n"
    "v_mfma_f32_16x16x32_bf16 a[176:179], v[100:103], v[48:51], a[176:179]\n"
    "v_exp_f32 v21, v21\n"
    "v_mfma_f32_16x16x32_bf16 a[180:183], v[100:103], v[52:55], a[180:183]\n"
    "ds_read_b128 v[80:83], v120 offset:24576\n"
    "v_add_f32 v117, v117, v20\n"
    "v_mfma_f32_16x16x32_bf16 a[180:183], v[96:99], v[36:39], a[180:183]\n"
    "ds_read_b128 v[84:87], v121 offset:24576\n"
    "v_add_f32 v117, v117, v21\n"
    "v_mfma_f32_16x16x32_bf16 a[184:187], v[96:99], v[40:43], a[184:187]\n"
    "ds_read_b128 v[88:91], v120 offset:25600\n"
    "v_cvt_pk_bf16_f32 v20, v20, v21\n"
    "v_mfma_f32_16x16x32_bf16 a[184:187], v[100:103], v[56:59], a[184:187]\n"
    "ds_read_b128 v[92:95], v121 offset:25600\n"
    "v_mfma_f32_16x16x32_bf16 a[188:191], v[100:103], v[60:63], a[188:191]\n"
    "v_mfma_f32_16x16x32_bf16 a[188:191], v[96:99], v[44:47], a[188:191]\n"
    "s_waitcnt lgkmcnt(4)\n"
    "v_mfma_f32_16x16x32_bf16 a[240:243], v[104:107], v[32:35], a[240:243]\n"
    "v_exp_f32 v22, v22\n"
    "v_mfma_f32_16x16x32_bf16 a[240:243], v[108:111], v[48:51], a[240:243]\n"
    "v_exp_f32 v23, v23\n"
    "v_mfma_f32_16x16x32_bf16 a[244:247], v[108:111], v[52:55], a[244:247]\n"
    "ds_read_b128 v[96:99], v120 offset:26624\n"
    "v_add_f32 v117, v117, v22\n"
    "v_mfma_f32_16x16x32_bf16 a[244:247], v[104:107], v[36:39], a[244:247]\n"
    "ds_read_b128 v[100:103], v121 offset:26624\n"
    "v_add_f32 v117, v117, v23\n"
    "v_mfma_f32_16x16x32_bf16 a[248:251], v[104:107], v[40:43], a[248:251]\n"
    "v_cvt_pk_bf16_f32 v21, v22, v23\n"
    "v_mfma_f32_16x16x32_bf16 a[248:251], v[108:111], v[56:59], a[248:251]\n"
    "v_mfma_f32_16x16x32_bf16 a[252:255], v[108:111], v[60:63], a[252:255]\n"
    "v_mfma_f32_16x16x32_bf16 a[252:255], v[104:107], v[44:47], a[252:255]\n"
    "s_waitcnt lgkmcnt(2)\n"
    "v_mfma_f32_16x16x32_bf16 v[64:67], v[80:83], v[128:131], v[112:115]\n"
    "v_exp_f32 v24, v24\n"
    "v_mfma_f32_16x16x32_bf16 v[64:67], v[84:87], v[132:135], v[64:67]\n"
    "v_exp_f32 v25, v25\n"
    "v_mfma_f32_16x16x32_bf16 v[64:67], v[88:91], v[136:139], v[64:67]\n"
    "ds_read_b128 v[104:107], v120 offset:27648\n"
    "v_add_f32 v118, v118, v24\n"
    "v_mfma_f32_16x16x32_bf16 v[64:67], v[92:95], v[140:143], v[64:67]\n"
    "ds_read_b128 v[108:111], v121 offset:27648\n"
    "v_add_f32 v118, v118, v25\n"
    "v_mfma_f32_16x16x32_bf16 v[68:71], v[92:95], v[172:175], v[112:115]\n"
    "v_cvt_pk_bf16_f32 v24, v24, v25\n"
    "s_add_u32 m0, s62, 0x4000\n"
    "v_mfma_f32_16x16x32_bf16 v[68:71], v[88:91], v[168:171], v[68:71]\n"
    "buffer_load_dwordx4 v123, s[44:47], s56 offen offset:0 nt lds\n"
    "v_mfma_f32_16x16x32_bf16 v[68:71], v[84:87], v[164:167], v[68:71]\n"
    "v_xor_b32 v122, 32, v122\n"
    "v_mfma_f32_16x16x32_bf16 v[68:71], v[80:83], v[160:163], v[68:71]\n"
    "v_mfma_f32_16x16x32_bf16 v[72:75], v[80:83], v[192:195], v[112:115]\n"
    "v_exp_f32 v26, v26\n"
    "v_mfma_f32_16x16x32_bf16 v[72:75], v[84:87], v[196:199], v[72:75]\n"
    "v_exp_f32 v27, v27\n"
    "v_mfma_f32_16x16x32_bf16 v[72:75], v[88:91], v[200:203], v[72:75]\n"
    "v_add_f32 v118, v118, v26\n"
    "v_mfma_f32_16x16x32_bf16 v[72:75], v[92:95], v[204:207], v[72:75]\n"
    "v_add_f32 v118, v118, v27\n"
    "v_mfma_f32_16x16x32_bf16 v[76:79], v[92:95], v[236:239], v[112:115]\n"
    "v_cvt_pk_bf16_f32 v25, v26, v27\n"
    "v_mfma_f32_16x16x32_bf16 v[76:79], v[88:91], v[232:235], v[76:79]\n"
    "v_mfma_f32_16x16x32_bf16 v[76:79], v[84:87], v[228:231], v[76:79]\n"
    "v_mfma_f32_16x16x32_bf16 v[76:79], v[80:83], v[224:227], v[76:79]\n"
    "s_waitcnt lgkmcnt(0)\n"
    "v_mfma_f32_16x16x32_bf16 v[64:67], v[96:99], v[144:147], v[64:67]\n"
    "v_exp_f32 v28, v28\n"
    "v_mfma_f32_16x16x32_bf16 v[64:67], v[100:103], v[148:151], v[64:67]\n"
    "v_exp_f32 v29, v29\n"
    "v_mfma_f32_16x16x32_bf16 v[64:67], v[104:107], v[152:155], v[64:67]\n"
    "ds_read_b64_tr_b16 v[80:81], v122 offset:0\n"
    "v_add_f32 v119, v119, v28\n"
    "v_mfma_f32_16x16x32_bf16 v[64:67], v[108:111], v[156:159], v[64:67]\n"
    "ds_read_b64_tr_b16 v[82:83], v122 offset:8192\n"
    "v_add_f32 v119, v119, v29\n"
    "v_mfma_f32_16x16x32_bf16 v[68:71], v[108:111], v[188:191], v[68:71]\n"
    "ds_read_b64_tr_b16 v[84:85], v122 offset:16384\n"
    "v_cvt_pk_bf16_f32 v28, v28, v29\n"
    "v_mfma_f32_16x16x32_bf16 v[68:71], v[104:107], v[184:187], v[68:71]\n"
    "ds_read_b64_tr_b16 v[86:87], v122 offset:24576\n"
    "s_add_u32 s62, s63, s61\n"
    "v_mfma_f32_16x16x32_bf16 v[68:71], v[100:103], v[180:183], v[68:71]\n"
    "buffer_load_dwordx4 v123, s[44:47], s57 offen offset:1024 nt lds\n"
    "v_mfma_f32_16x16x32_bf16 v[68:71], v[96:99], v[176:179], v[68:71]\n"
    "s_add_u32 s63, s63, 0x8000\n"
    "s_cmp_eq_u32 s63, 0x18000\n"
    "v_mfma_f32_16x16x32_bf16 v[72:75], v[96:99], v[208:211], v[72:75]\n"
    "v_exp_f32 v30, v30\n"
    "v_mfma_f32_16x16x32_bf16 v[72:75], v[100:103], v[212:215], v[72:75]\n"
    "v_exp_f32 v31, v31\n"
    "v_mfma_f32_16x16x32_bf16 v[72:75], v[104:107], v[216:219], v[72:75]\n"
    "ds_read_b64_tr_b16 v[88:89], v122 offset:1024\n"
    "v_add_f32 v119, v119, v30\n"
    "v_mfma_f32_16x16x32_bf16 v[72:75], v[108:111], v[220:223], v[72:75]\n"
    "ds_read_b64_tr_b16 v[90:91], v122 offset:9216\n"
    "v_add_f32 v119, v119, v31\n"
    "v_mfma_f32_16x16x32_bf16 v[76:79], v[108:111], v[252:255], v[76:79]\n"
    "ds_read_b64_tr_b16 v[92:93], v122 offset:17408\n"
    "v_cvt_pk_bf16_f32 v29, v30, v31\n"
    "v_mfma_f32_16x16x32_bf16 v[76:79], v[104:107], v[248:251], v[76:79]\n"
    "ds_read_b64_tr_b16 v[94:95], v122 offset:25600\n"
    "s_cselect_b32 s64, s68, s67\n"
    "v_mfma_f32_16x16x32_bf16 v[76:79], v[100:103], v[244:247], v[76:79]\n"
    "buffer_load_dwordx4 v123, s[44:47], s58 offen offset:2048 nt lds\n"
    "v_mfma_f32_16x16x32_bf16 v[76:79], v[96:99], v[240:243], v[76:79]\n"
    "s_cselect_b32 s63, 0, s63\n"
    "s_waitcnt lgkmcnt(4)\n"
    "v_mfma_f32_16x16x32_bf16 a[32:35], v[80:83], v[32:35], a[32:35]\n"
    "v_exp_f32 v64, v64\n"
    "v_mfma_f32_16x16x32_bf16 a[32:35], v[84:87], v[48:51], a[32:35]\n"
    "v_exp_f32 v65, v65\n"
    "v_mfma_f32_16x16x32_bf16 a[36:39], v[84:87], v[52:55], a[36:39]\n"
    "ds_read_b64_tr_b16 v[96:97], v122 offset:2048\n"
    "v_add_f32 v116, v116, v64\n"
    "v_mfma_f32_16x16x32_bf16 a[36:39], v[80:83], v[36:39], a[36:39]\n"
    "ds_read_b64_tr_b16 v[98:99], v122 offset:10240\n"
    "v_add_f32 v116, v116, v65\n"
    "v_mfma_f32_16x16x32_bf16 a[40:43], v[80:83], v[40:43], a[40:43]\n"
    "ds_read_b64_tr_b16 v[100:101], v122 offset:18432\n"
    "v_cvt_pk_bf16_f32 v18, v64, v65\n"
    "v_mfma_f32_16x16x32_bf16 a[40:43], v[84:87], v[56:59], a[40:43]\n"
    "ds_read_b64_tr_b16 v[102:103], v122 offset:26624\n"
    "v_mfma_f32_16x16x32_bf16 a[44:47], v[84:87], v[60:63], a[44:47]\n"
    "v_mfma_f32_16x16x32_bf16 a[44:47], v[80:83], v[44:47], a[44:47]\n"
    "s_waitcnt lgkmcnt(4)\n"
    "v_mfma_f32_16x16x32_bf16 a[96:99], v[88:91], v[32:35], a[96:99]\n"
    "v_exp_f32 v66, v66\n"
    "v_mfma_f32_16x16x32_bf16 a[96:99], v[92:95], v[48:51], a[96:99]\n"
    "v_exp_f32 v67, v67\n"
    "v_mfma_f32_16x16x32_bf16 a[100:103], v[92:95], v[52:55], a[100:103]\n"
    "ds_read_b64_tr_b16 v[104:105], v122 offset:3072\n"
    "v_add_f32 v116, v116, v66\n"
    "v_mfma_f32_16x16x32_bf16 a[100:103], v[88:91], v[36:39], a[100:103]\n"
    "ds_read_b64_tr_b16 v[106:107], v122 offset:11264\n"
    "v_add_f32 v116, v116, v67\n"
    "v_mfma_f32_16x16x32_bf16 a[104:107], v[88:91], v[40:43], a[104:107]\n"
    "ds_read_b64_tr_b16 v[108:109], v122 offset:19456\n"
    "v_cvt_pk_bf16_f32 v19, v66, v67\n"
    "v_mfma_f32_16x16x32_bf16 a[104:107], v[92:95], v[56:59], a[104:107]\n"
    "ds_read_b64_tr_b16 v[110:111], v122 offset:27648\n"
    "v_mfma_f32_16x16x32_bf16 a[108:111], v[92:95], v[60:63], a[108:111]\n"
    "buffer_load_dwordx4 v123, s[44:47], s59 offen offset:3072 nt lds\n"
    "v_mfma_f32_16x16x32_bf16 a[108:111], v[88:91], v[44:47], a[108:111]\n"
    "s_waitcnt lgkmcnt(4)\n"
    "v_mfma_f32_16x16x32_bf16 a[160:163], v[96:99], v[32:35], a[160:163]\n"
    "v_exp_f32 v68, v68\n"
    "v_mfma_f32_16x16x32_bf16 a[160:163], v[100:103], v[48:51], a[160:163]\n"
    "v_exp_f32 v69, v69\n"
    "v_mfma_f32_16x16x32_bf16 a[164:167], v[100:103], v[52:55], a[164:167]\n"
    "ds_read_b128 v[80:83], v120 offset:32768\n"
    "v_add_f32 v117, v117, v68\n"
    "v_mfma_f32_16x16x32_bf16 a[164:167], v[96:99], v[36:39], a[164:167]\n"
    "ds_read_b128 v[84:87], v121 offset:32768\n"
    "v_add_f32 v117, v117, v69\n"
    "v_mfma_f32_16x16x32_bf16 a[168:171], v[96:99], v[40:43], a[168:171]\n"
    "ds_read_b128 v[88:91], v120 offset:33792\n"
    "v_cvt_pk_bf16_f32 v22, v68, v69\n"
    "v_mfma_f32_16x16x32_bf16 a[168:171], v[100:103], v[56:59], a[168:171]\n"
    "ds_read_b128 v[92:95], v121 offset:33792\n"
    "v_mfma_f32_16x16x32_bf16 a[172:175], v[100:103], v[60:63], a[172:175]\n"
    "v_mfma_f32_16x16x32_bf16 a[172:175], v[96:99], v[44:47], a[172:175]\n"
    "s_waitcnt lgkmcnt(4)\n"
    "v_mfma_f32_16x16x32_bf16 a[224:227], v[104:107], v[32:35], a[224:227]\n"
    "v_exp_f32 v70, v70\n"
    "v_mfma_f32_16x16x32_bf16 a[224:227], v[108:111], v[48:51], a[224:227]\n"
    "v_exp_f32 v71, v71\n"
    "v_mfma_f32_16x16x32_bf16 a[228:231], v[108:111], v[52:55], a[228:231]\n"
    "ds_read_b128 v[96:99], v120 offset:34816\n"
    "v_add_f32 v117, v117, v70\n"
    "v_mfma_f32_16x16x32_bf16 a[228:231], v[104:107], v[36:39], a[228:231]\n"
    "ds_read_b128 v[100:103], v121 offset:34816\n"
    "v_add_f32 v117, v117, v71\n"
    "v_mfma_f32_16x16x32_bf16 a[232:235], v[104:107], v[40:43], a[232:235]\n"
    "v_cvt_pk_bf16_f32 v23, v70, v71\n"
    "v_mfma_f32_16x16x32_bf16 a[232:235], v[108:111], v[56:59], a[232:235]\n"
    "v_mfma_f32_16x16x32_bf16 a[236:239], v[108:111], v[60:63], a[236:239]\n"
    "s_waitcnt vmcnt(13) lgkmcnt(2)\n"
    "v_mfma_f32_16x16x32_bf16 a[236:239], v[104:107], v[44:47], a[236:239]\n"
    "s_barrier\n"
    "v_mfma_f32_16x16x32_bf16 v[32:35], v[80:83], v[128:131], v[112:115]\n"
    "v_exp_f32 v72, v72\n"
    "v_mfma_f32_16x16x32_bf16 v[32:35], v[84:87], v[132:135], v[32:35]\n"
    "v_exp_f32 v73, v73\n"
    "v_mfma_f32_16x16x32_bf16 v[32:35], v[88:91], v[136:139], v[32:35]\n"
    "ds_read_b128 v[104:107], v120 offset:35840\n"
    "v_add_f32 v118, v118, v72\n"
    "v_mfma_f32_16x16x32_bf16 v[32:35], v[92:95], v[140:143], v[32:35]\n"
    "ds_read_b128 v[108:111], v121 offset:35840\n"
    "v_add_f32 v118, v118, v73\n"
    "v_mfma_f32_16x16x32_bf16 v[36:39], v[92:95], v[172:175], v[112:115]\n"
    "v_cvt_pk_bf16_f32 v26, v72, v73\n"
    "v_add_u32 v123, 0x4000, v123\n"
    "v_mfma_f32_16x16x32_bf16 v[36:39], v[88:91], v[168:171], v[36:39]\n"
    "s_add_u32 m0, s60, 0x4000\n"
    "v_add_u32 v122, s64, v122\n"
    "v_mfma_f32_16x16x32_bf16 v[36:39], v[84:87], v[164:167], v[36:39]\n"
    "buffer_load_dwordx4 v123, s[40:43], s56 offen offset:0 nt lds\n"
    "v_mfma_f32_16x16x32_bf16 v[36:39], v[80:83], v[160:163], v[36:39]\n"
    "v_xor_b32 v122, 64, v122\n"
    "v_mfma_f32_16x16x32_bf16 v[40:43], v[80:83], v[192:195], v[112:115]\n"
    "v_exp_f32 v74, v74\n"
    "v_mfma_f32_16x16x32_bf16 v[40:43], v[84:87], v[196:199], v[40:43]\n"
    "v_exp_f32 v75, v75\n"
    "v_mfma_f32_16x16x32_bf16 v[40:43], v[88:91], v[200:203], v[40:43]\n"
    "v_add_f32 v118, v118, v74\n"
    "v_mfma_f32_16x16x32_bf16 v[40:43], v[92:95], v[204:207], v[40:43]\n"
    "v_add_f32 v118, v118, v75\n"
    "v_mfma_f32_16x16x32_bf16 v[44:47], v[92:95], v[236:239], v[112:115]\n"
    "v_cvt_pk_bf16_f32 v27, v74, v75\n"
    "v_mfma_f32_16x16x32_bf16 v[44:47], v[88:91], v[232:235], v[44:47]\n"
    "v_mfma_f32_16x16x32_bf16 v[44:47], v[84:87], v[228:231], v[44:47]\n"
    "v_mfma_f32_16x16x32_bf16 v[44:47], v[80:83], v[224:227], v[44:47]\n"
    "s_waitcnt lgkmcnt(0)\n"
    "v_mfma_f32_16x16x32_bf16 v[32:35], v[96:99], v[144:147], v[32:35]\n"
    "v_exp_f32 v76, v76\n"
    "v_mfma_f32_16x16x32_bf16 v[32:35], v[100:103], v[148:151], v[32:35]\n"
    "v_exp_f32 v77, v77\n"
    "v_mfma_f32_16x16x32_bf16 v[32:35], v[104:107], v[152:155], v[32:35]\n"
    "ds_read_b64_tr_b16 v[80:81], v122 offset:0\n"
    "v_add_f32 v119, v119, v76\n"
    "v_mfma_f32_16x16x32_bf16 v[32:35], v[108:111], v[156:159], v[32:35]\n"
    "ds_read_b64_tr_b16 v[82:83], v122 offset:8192\n"
    "v_add_f32 v119, v119, v77\n"
    "v_mfma_f32_16x16x32_bf16 v[36:39], v[108:111], v[188:191], v[36:39]\n"
    "ds_read_b64_tr_b16 v[84:85], v122 offset:16384\n"
    "v_cvt_pk_bf16_f32 v30, v76, v77\n"
    "v_mfma_f32_16x16x32_bf16 v[36:39], v[104:107], v[184:187], v[36:39]\n"
    "ds_read_b64_tr_b16 v[86:87], v122 offset:24576\n"
    "v_mfma_f32_16x16x32_bf16 v[36:39], v[100:103], v[180:183], v[36:39]\n"
    "buffer_load_dwordx4 v123, s[40:43], s57 offen offset:1024 nt lds\n"
    "v_mfma_f32_16x16x32_bf16 v[36:39], v[96:99], v[176:179], v[36:39]\n"
    "v_mfma_f32_16x16x32_bf16 v[40:43], v[96:99], v[208:211], v[40:43]\n"
    "v_exp_f32 v78, v78\n"
    "v_mfma_f32_16x16x32_bf16 v[40:43], v[100:103], v[212:215], v[40:43]\n"
    "v_exp_f32 v79, v79\n"
    "v_mfma_f32_16x16x32_bf16 v[40:43], v[104:107], v[216:219], v[40:43]\n"
    "ds_read_b64_tr_b16 v[88:89], v122 offset:1024\n"
    "v_add_f32 v119, v119, v78\n"
    "v_mfma_f32_16x16x32_bf16 v[40:43], v[108:111], v[220:223], v[40:43]\n"
    "ds_read_b64_tr_b16 v[90:91], v122 offset:9216\n"
    "v_add_f32 v119, v119, v79\n"
    "v_mfma_f32_16x16x32_bf16 v[44:47], v[108:111], v[252:255], v[44:47]\n"
    "ds_read_b64_tr_b16 v[92:93], v122 offset:17408\n"
    "v_cvt_pk_bf16_f32 v31, v78, v79\n"
    "v_mfma_f32_16x16x32_bf16 v[44:47], v[104:107], v[248:251], v[44:47]\n"
    "ds_read_b64_tr_b16 v[94:95], v122 offset:25600\n"
    "v_mfma_f32_16x16x32_bf16 v[44:47], v[100:103], v[244:247], v[44:47]\n"
    "buffer_load_dwordx4 v123, s[40:43], s58 offen offset:2048 nt lds\n"
    "v_mfma_f32_16x16x32_bf16 v[44:47], v[96:99], v[240:243], v[44:47]\n"
    "s_waitcnt lgkmcnt(4)\n"
    "v_mfma_f32_16x16x32_bf16 a[0:3], v[80:83], v[0:3], a[0:3]\n"
    "v_exp_f32 v32, v32\n"
    "v_mfma_f32_16x16x32_bf16 a[0:3], v[84:87], v[16:19], a[0:3]\n"
    "v_exp_f32 v33, v33\n"
    "v_mfma_f32_16x16x32_bf16 a[4:7], v[84:87], v[20:23], a[4:7]\n"
    "ds_read_b64_tr_b16 v[96:97], v122 offset:2048\n"
    "v_add_f32 v116, v116, v32\n"
    "v_mfma_f32_16x16x32_bf16 a[4:7], v[80:83], v[4:7], a[4:7]\n"
    "ds_read_b64_tr_b16 v[98:99], v122 offset:10240\n"
    "v_add_f32 v116, v116, v33\n"
    "v_mfma_f32_16x16x32_bf16 a[8:11], v[80:83], v[8:11], a[8:11]\n"
    "ds_read_b64_tr_b16 v[100:101], v122 offset:18432\n"
    "v_cvt_pk_bf16_f32 v32, v32, v33\n"
    "v_mfma_f32_16x16x32_bf16 a[8:11], v[84:87], v[24:27], a[8:11]\n"
    "ds_read_b64_tr_b16 v[102:103], v122 offset:26624\n"
    "v_mfma_f32_16x16x32_bf16 a[12:15], v[84:87], v[28:31], a[12:15]\n"
    "v_mfma_f32_16x16x32_bf16 a[12:15], v[80:83], v[12:15], a[12:15]\n"
    "s_waitcnt lgkmcnt(4)\n"
    "v_mfma_f32_16x16x32_bf16 a[64:67], v[88:91], v[0:3], a[64:67]\n"
    "v_exp_f32 v34, v34\n"
    "v_mfma_f32_16x16x32_bf16 a[64:67], v[92:95], v[16:19], a[64:67]\n"
    "v_exp_f32 v35, v35\n"
    "v_mfma_f32_16x16x32_bf16 a[68:71], v[92:95], v[20:23], a[68:71]\n"
    "ds_read_b64_tr_b16 v[104:105], v122 offset:3072\n"
    "v_add_f32 v116, v116, v34\n"
    "v_mfma_f32_16x16x32_bf16 a[68:71], v[88:91], v[4:7], a[68:71]\n"
    "ds_read_b64_tr_b16 v[106:107], v122 offset:11264\n"
    "v_add_f32 v116, v116, v35\n"
    "v_mfma_f32_16x16x32_bf16 a[72:75], v[88:91], v[8:11], a[72:75]\n"
    "ds_read_b64_tr_b16 v[108:109], v122 offset:19456\n"
    "v_cvt_pk_bf16_f32 v33, v34, v35\n"
    "v_mfma_f32_16x16x32_bf16 a[72:75], v[92:95], v[24:27], a[72:75]\n"
    "ds_read_b64_tr_b16 v[110:111], v122 offset:27648\n"
    "v_mfma_f32_16x16x32_bf16 a[76:79], v[92:95], v[28:31], a[76:79]\n"
    "buffer_load_dwordx4 v123, s[40:43], s59 offen offset:3072 nt lds\n"
    "v_mfma_f32_16x16x32_bf16 a[76:79], v[88:91], v[12:15], a[76:79]\n"
    "s_waitcnt lgkmcnt(4)\n"
    "v_mfma_f32_16x16x32_bf16 a[128:131], v[96:99], v[0:3], a[128:131]\n"
    "v_exp_f32 v36, v36\n"
    "v_mfma_f32_16x16x32_bf16 a[128:131], v[100:103], v[16:19], a[128:131]\n"
    "v_exp_f32 v37, v37\n"
    "v_mfma_f32_16x16x32_bf16 a[132:135], v[100:103], v[20:23], a[132:135]\n"
    "ds_read_b128 v[80:83], v120 offset:40960\n"
    "v_add_f32 v117, v117, v36\n"
    "v_mfma_f32_16x16x32_bf16 a[132:135], v[96:99], v[4:7], a[132:135]\n"
    "ds_read_b128 v[84:87], v121 offset:40960\n"
    "v_add_f32 v117, v117, v37\n"
    "v_mfma_f32_16x16x32_bf16 a[136:139], v[96:99], v[8:11], a[136:139]\n"
    "ds_read_b128 v[88:91], v120 offset:41984\n"
    "v_cvt_pk_bf16_f32 v36, v36, v37\n"
    "v_mfma_f32_16x16x32_bf16 a[136:139], v[100:103], v[24:27], a[136:139]\n"
    "ds_read_b128 v[92:95], v121 offset:41984\n"
    "v_mfma_f32_16x16x32_bf16 a[140:143], v[100:103], v[28:31], a[140:143]\n"
    "v_mfma_f32_16x16x32_bf16 a[140:143], v[96:99], v[12:15], a[140:143]\n"
    "s_waitcnt lgkmcnt(4)\n"
    "v_mfma_f32_16x16x32_bf16 a[192:195], v[104:107], v[0:3], a[192:195]\n"
    "v_exp_f32 v38, v38\n"
    "v_mfma_f32_16x16x32_bf16 a[192:195], v[108:111], v[16:19], a[192:195]\n"
    "v_exp_f32 v39, v39\n"
    "v_mfma_f32_16x16x32_bf16 a[196:199], v[108:111], v[20:23], a[196:199]\n"
    "ds_read_b128 v[96:99], v120 offset:43008\n"
    "v_add_f32 v117, v117, v38\n"
    "v_mfma_f32_16x16x32_bf16 a[196:199], v[104:107], v[4:7], a[196:199]\n"
    "ds_read_b128 v[100:103], v121 offset:43008\n"
    "v_add_f32 v117, v117, v39\n"
    "v_mfma_f32_16x16x32_bf16 a[200:203], v[104:107], v[8:11], a[200:203]\n"
    "v_cvt_pk_bf16_f32 v37, v38, v39\n"
    "v_mfma_f32_16x16x32_bf16 a[200:203], v[108:111], v[24:27], a[200:203]\n"
    "v_mfma_f32_16x16x32_bf16 a[204:207], v[108:111], v[28:31], a[204:207]\n"
    "v_mfma_f32_16x16x32_bf16 a[204:207], v[104:107], v[12:15], a[204:207]\n"
    "s_waitcnt lgkmcnt(2)\n"
    "v_mfma_f32_16x16x32_bf16 v[64:67], v[80:83], v[128:131], v[112:115]\n"
    "v_exp_f32 v40, v40\n"
    "v_mfma_f32_16x16x32_bf16 v[64:67], v[84:87], v[132:135], v[64:67]\n"
    "v_exp_f32 v41, v41\n"
    "v_mfma_f32_16x16x32_bf16 v[64:67], v[88:91], v[136:139], v[64:67]\n"
    "ds_read_b128 v[104:107], v120 offset:44032\n"
    "v_add_f32 v118, v118, v40\n"
    "v_mfma_f32_16x16x32_bf16 v[64:67], v[92:95], v[140:143], v[64:67]\n"
    "ds_read_b128 v[108:111], v121 offset:44032\n"
    "v_add_f32 v118, v118, v41\n"
    "v_mfma_f32_16x16x32_bf16 v[68:71], v[92:95], v[172:175], v[112:115]\n"
    "v_cvt_pk_bf16_f32 v40, v40, v41\n"
    "s_add_u32 m0, s62, 0x0\n"
    "v_mfma_f32_16x16x32_bf16 v[68:71], v[88:91], v[168:171], v[68:71]\n"
    "buffer_load_dwordx4 v123, s[44:47], s56 offen offset:0 nt lds\n"
    "v_mfma_f32_16x16x32_bf16 v[68:71], v[84:87], v[164:167], v[68:71]\n"
    "v_xor_b32 v122, 32, v122\n"
    "v_mfma_f32_16x16x32_bf16 v[68:71], v[80:83], v[160:163], v[68:71]\n"
    "v_mfma_f32_16x16x32_bf16 v[72:75], v[80:83], v[192:195], v[112:115]\n"
    "v_exp_f32 v42, v42\n"
    "v_mfma_f32_16x16x32_bf16 v[72:75], v[84:87], v[196:199], v[72:75]\n"
    "v_exp_f32 v43, v43\n"
    "v_mfma_f32_16x16x32_bf16 v[72:75], v[88:91], v[200:203], v[72:75]\n"
    "v_add_f32 v118, v118, v42\n"
    "v_mfma_f32_16x16x32_bf16 v[72:75], v[92:95], v[204:207], v[72:75]\n"
    "v_add_f32 v118, v118, v43\n"
    "v_mfma_f32_16x16x32_bf16 v[76:79], v[92:95], v[236:239], v[112:115]\n"
    "v_cvt_pk_bf16_f32 v41, v42, v43\n"
    "v_mfma_f32_16x16x32_bf16 v[76:79], v[88:91], v[232:235], v[76:79]\n"
    "v_mfma_f32_16x16x32_bf16 v[76:79], v[84:87], v[228:231], v[76:79]\n"
    "v_mfma_f32_16x16x32_bf16 v[76:79], v[80:83], v[224:227], v[76:79]\n"
    "s_waitcnt lgkmcnt(0)\n"
    "v_mfma_f32_16x16x32_bf16 v[64:67], v[96:99], v[144:147], v[64:67]\n"
    "v_exp_f32 v44, v44\n"
    "v_mfma_f32_16x16x32_bf16 v[64:67], v[100:103], v[148:151], v[64:67]\n"
    "v_exp_f32 v45, v45\n"
    "v_mfma_f32_16x16x32_bf16 v[64:67], v[104:107], v[152:155], v[64:67]\n"
    "ds_read_b64_tr_b16 v[80:81], v122 offset:0\n"
    "v_add_f32 v119, v119, v44\n"
    "v_mfma_f32_16x16x32_bf16 v[64:67], v[108:111], v[156:159], v[64:67]\n"
    "ds_read_b64_tr_b16 v[82:83], v122 offset:8192\n"
    "v_add_f32 v119, v119, v45\n"
    "v_mfma_f32_16x16x32_bf16 v[68:71], v[108:111], v[188:191], v[68:71]\n"
    "ds_read_b64_tr_b16 v[84:85], v122 offset:16384\n"
    "v_cvt_pk_bf16_f32 v44, v44, v45\n"
    "v_mfma_f32_16x16x32_bf16 v[68:71], v[104:107], v[184:187], v[68:71]\n"
    "ds_read_b64_tr_b16 v[86:87], v122 offset:24576\n"
    "v_mfma_f32_16x16x32_bf16 v[68:71], v[100:103], v[180:183], v[68:71]\n"
    "buffer_load_dwordx4 v123, s[44:47], s57 offen offset:1024 nt lds\n"
    "v_mfma_f32_16x16x32_bf16 v[68:71], v[96:99], v[176:179], v[68:71]\n"
    "v_mfma_f32_16x16x32_bf16 v[72:75], v[96:99], v[208:211], v[72:75]\n"
    "v_exp_f32 v46, v46\n"
    "v_mfma_f32_16x16x32_bf16 v[72:75], v[100:103], v[212:215], v[72:75]\n"
    "v_exp_f32 v47, v47\n"
    "v_mfma_f32_16x16x32_bf16 v[72:75], v[104:107], v[216:219], v[72:75]\n"
    "ds_read_b64_tr_b16 v[88:89], v122 offset:1024\n"
    "v_add_f32 v119, v119, v46\n"
    "v_mfma_f32_16x16x32_bf16 v[72:75], v[108:111], v[220:223], v[72:75]\n"
    "ds_read_b64_tr_b16 v[90:91], v122 offset:9216\n"
    "v_add_f32 v119, v119, v47\n"
    "v_mfma_f32_16x16x32_bf16 v[76:79], v[108:111], v[252:255], v[76:79]\n"
    "ds_read_b64_tr_b16 v[92:93], v122 offset:17408\n"
    "v_cvt_pk_bf16_f32 v45, v46, v47\n"
    "v_mfma_f32_16x16x32_bf16 v[76:79], v[104:107], v[248:251], v[76:79]\n"
    "ds_read_b64_tr_b16 v[94:95], v122 offset:25600\n"
    "v_mfma_f32_16x16x32_bf16 v[76:79], v[100:103], v[244:247], v[76:79]\n"
    "buffer_load_dwordx4 v123, s[44:47], s58 offen offset:2048 nt lds\n"
    "v_mfma_f32_16x16x32_bf16 v[76:79], v[96:99], v[240:243], v[76:79]\n"
    "s_waitcnt lgkmcnt(4)\n"
    "v_mfma_f32_16x16x32_bf16 a[16:19], v[80:83], v[0:3], a[16:19]\n"
    "v_exp_f32 v64, v64\n"
    "v_mfma_f32_16x16x32_bf16 a[16:19], v[84:87], v[16:19], a[16:19]\n"
    "v_exp_f32 v65, v65\n"
    "v_mfma_f32_16x16x32_bf16 a[20:23], v[84:87], v[20:23], a[20:23]\n"
    "ds_read_b64_tr_b16 v[96:97], v122 offset:2048\n"
    "v_add_f32 v116, v116, v64\n"
    "v_mfma_f32_16x16x32_bf16 a[20:23], v[80:83], v[4:7], a[20:23]\n"
    "ds_read_b64_tr_b16 v[98:99], v122 offset:10240\n"
    "v_add_f32 v116, v116, v65\n"
    "v_mfma_f32_16x16x32_bf16 a[24:27], v[80:83], v[8:11], a[24:27]\n"
    "ds_read_b64_tr_b16 v[100:101], v122 offset:18432\n"
    "v_cvt_pk_bf16_f32 v34, v64, v65\n"
    "v_mfma_f32_16x16x32_bf16 a[24:27], v[84:87], v[24:27], a[24:27]\n"
    "ds_read_b64_tr_b16 v[102:103], v122 offset:26624\n"
    "v_mfma_f32_16x16x32_bf16 a[28:31], v[84:87], v[28:31], a[28:31]\n"
    "v_mfma_f32_16x16x32_bf16 a[28:31], v[80:83], v[12:15], a[28:31]\n"
    "s_waitcnt lgkmcnt(4)\n"
    "v_mfma_f32_16x16x32_bf16 a[80:83], v[88:91], v[0:3], a[80:83]\n"
    "v_exp_f32 v66, v66\n"
    "v_mfma_f32_16x16x32_bf16 a[80:83], v[92:95], v[16:19], a[80:83]\n"
    "v_exp_f32 v67, v67\n"
    "v_mfma_f32_16x16x32_bf16 a[84:87], v[92:95], v[20:23], a[84:87]\n"
    "ds_read_b64_tr_b16 v[104:105], v122 offset:3072\n"
    "v_add_f32 v116, v116, v66\n"
    "v_mfma_f32_16x16x32_bf16 a[84:87], v[88:91], v[4:7], a[84:87]\n"
    "ds_read_b64_tr_b16 v[106:107], v122 offset:11264\n"
    "v_add_f32 v116, v116, v67\n"
    "v_mfma_f32_16x16x32_bf16 a[88:91], v[88:91], v[8:11], a[88:91]\n"
    "ds_read_b64_tr_b16 v[108:109], v122 offset:19456\n"
    "v_cvt_pk_bf16_f32 v35, v66, v67\n"
    "v_mfma_f32_16x16x32_bf16 a[88:91], v[92:95], v[24:27], a[88:91]\n"
    "ds_read_b64_tr_b16 v[110:111], v122 offset:27648\n"
    "v_mfma_f32_16x16x32_bf16 a[92:95], v[92:95], v[28:31], a[92:95]\n"
    "buffer_load_dwordx4 v123, s[44:47], s59 offen offset:3072 nt lds\n"
    "v_mfma_f32_16x16x32_bf16 a[92:95], v[88:91], v[12:15], a[92:95]\n"
    "s_waitcnt lgkmcnt(4)\n"
    "v_mfma_f32_16x16x32_bf16 a[144:147], v[96:99], v[0:3], a[144:147]\n"
    "v_exp_f32 v68, v68\n"
    "v_mfma_f32_16x16x32_bf16 a[144:147], v[100:103], v[16:19], a[144:147]\n"
    "v_exp_f32 v69, v69\n"
    "v_mfma_f32_16x16x32_bf16 a[148:151], v[100:103], v[20:23], a[148:151]\n"
    "ds_read_b128 v[80:83], v120 offset:49152\n"
    "v_add_f32 v117, v117, v68\n"
    "v_mfma_f32_16x16x32_bf16 a[148:151], v[96:99], v[4:7], a[148:151]\n"
    "ds_read_b128 v[84:87], v121 offset:49152\n"
    "v_add_f32 v117, v117, v69\n"
    "v_mfma_f32_16x16x32_bf16 a[152:155], v[96:99], v[8:11], a[152:155]\n"
    "ds_read_b128 v[88:91], v120 offset:50176\n"
    "v_cvt_pk_bf16_f32 v38, v68, v69\n"
    "v_mfma_f32_16x16x32_bf16 a[152:155], v[100:103], v[24:27], a[152:155]\n"
    "ds_read_b128 v[92:95], v121 offset:50176\n"
    "v_mfma_f32_16x16x32_bf16 a[156:159], v[100:103], v[28:31], a[156:159]\n"
    "v_mfma_f32_16x16x32_bf16 a[156:159], v[96:99], v[12:15], a[156:159]\n"
    "s_waitcnt lgkmcnt(4)\n"
    "v_mfma_f32_16x16x32_bf16 a[208:211], v[104:107], v[0:3], a[208:211]\n"
    "v_exp_f32 v70, v70\n"
    "v_mfma_f32_16x16x32_bf16 a[208:211], v[108:111], v[16:19], a[208:211]\n"
    "v_exp_f32 v71, v71\n"
    "v_mfma_f32_16x16x32_bf16 a[212:215], v[108:111], v[20:23], a[212:215]\n"
    "ds_read_b128 v[96:99], v120 offset:51200\n"
    "v_add_f32 v117, v117, v70\n"
    "v_mfma_f32_16x16x32_bf16 a[212:215], v[104:107], v[4:7], a[212:215]\n"
    "ds_read_b128 v[100:103], v121 offset:51200\n"
    "v_add_f32 v117, v117, v71\n"
    "v_mfma_f32_16x16x32_bf16 a[216:219], v[104:107], v[8:11], a[216:219]\n"
    "v_cvt_pk_bf16_f32 v39, v70, v71\n"
    "v_mfma_f32_16x16x32_bf16 a[216:219], v[108:111], v[24:27], a[216:219]\n"
    "v_mfma_f32_16x16x32_bf16 a[220:223], v[108:111], v[28:31], a[220:223]\n"
    "s_waitcnt vmcnt(12) lgkmcnt(2)\n"
    "v_mfma_f32_16x16x32_bf16 a[220:223], v[104:107], v[12:15], a[220:223]\n"
    "s_barrier\n"
    "v_mfma_f32_16x16x32_bf16 v[48:51], v[80:83], v[128:131], v[112:115]\n"
    "v_exp_f32 v72, v72\n"
    "v_mfma_f32_16x16x32_bf16 v[48:51], v[84:87], v[132:135], v[48:51]\n"
    "v_exp_f32 v73, v73\n"
    "v_mfma_f32_16x16x32_bf16 v[48:51], v[88:91], v[136:139], v[48:51]\n"
    "ds_read_b128 v[104:107], v120 offset:52224\n"
    "v_add_f32 v118, v118, v72\n"
    "v_mfma_f32_16x16x32_bf16 v[48:51], v[92:95], v[140:143], v[48:51]\n"
    "ds_read_b128 v[108:111], v121 offset:52224\n"
    "v_add_f32 v118, v118, v73\n"
    "v_mfma_f32_16x16x32_bf16 v[52:55], v[92:95], v[172:175], v[112:115]\n"
    "v_cvt_pk_bf16_f32 v42, v72, v73\n"
    "v_add_u32 v123, 0x4000, v123\n"
    "v_mfma_f32_16x16x32_bf16 v[52:55], v[88:91], v[168:171], v[52:55]\n"
    "s_add_u32 m0, s60, 0x8000\n"
    "v_xor_b32 v122, 64, v122\n"
    "v_mfma_f32_16x16x32_bf16 v[52:55], v[84:87], v[164:167], v[52:55]\n"
    "buffer_load_dwordx4 v123, s[40:43], s56 offen offset:0 nt lds\n"
    "v_mfma_f32_16x16x32_bf16 v[52:55], v[80:83], v[160:163], v[52:55]\n"
    "v_mfma_f32_16x16x32_bf16 v[56:59], v[80:83], v[192:195], v[112:115]\n"
    "v_exp_f32 v74, v74\n"
    "v_mfma_f32_16x16x32_bf16 v[56:59], v[84:87], v[196:199], v[56:59]\n"
    "v_exp_f32 v75, v75\n"
    "v_mfma_f32_16x16x32_bf16 v[56:59], v[88:91], v[200:203], v[56:59]\n"
    "v_add_f32 v118, v118, v74\n"
    "v_mfma_f32_16x16x32_bf16 v[56:59], v[92:95], v[204:207], v[56:59]\n"
    "v_add_f32 v118, v118, v75\n"
    "v_mfma_f32_16x16x32_bf16 v[60:63], v[92:95], v[236:239], v[112:115]\n"
    "v_cvt_pk_bf16_f32 v43, v74, v75\n"
    "v_mfma_f32_16x16x32_bf16 v[60:63], v[88:91], v[232:235], v[60:63]\n"
    "v_mfma_f32_16x16x32_bf16 v[60:63], v[84:87], v[228:231], v[60:63]\n"
    "v_mfma_f32_16x16x32_bf16 v[60:63], v[80:83], v[224:227], v[60:63]\n"
    "s_waitcnt lgkmcnt(0)\n"
    "v_mfma_f32_16x16x32_bf16 v[48:51], v[96:99], v[144:147], v[48:51]\n"
    "v_exp_f32 v76, v76\n"
    "v_mfma_f32_16x16x32_bf16 v[48:51], v[100:103], v[148:151], v[48:51]\n"
    "v_exp_f32 v77, v77\n"
    "v_mfma_f32_16x16x32_bf16 v[48:51], v[104:107], v[152:155], v[48:51]\n"
    "ds_read_b64_tr_b16 v[80:81], v122 offset:0\n"
    "v_add_f32 v119, v119, v76\n"
    "v_mfma_f32_16x16x32_bf16 v[48:51], v[108:111], v[156:159], v[48:51]\n"
    "ds_read_b64_tr_b16 v[82:83], v122 offset:8192\n"
    "v_add_f32 v119, v119, v77\n"
    "v_mfma_f32_16x16x32_bf16 v[52:55], v[108:111], v[188:191], v[52:55]\n"
    "ds_read_b64_tr_b16 v[84:85], v122 offset:16384\n"
    "v_cvt_pk_bf16_f32 v46, v76, v77\n"
    "v_mfma_f32_16x16x32_bf16 v[52:55], v[104:107], v[184:187], v[52:55]\n"
    "ds_read_b64_tr_b16 v[86:87], v122 offset:24576\n"
    "v_mfma_f32_16x16x32_bf16 v[52:55], v[100:103], v[180:183], v[52:55]\n"
    "buffer_load_dwordx4 v123, s[40:43], s57 offen offset:1024 nt lds\n"
    "v_mfma_f32_16x16x32_bf16 v[52:55], v[96:99], v[176:179], v[52:55]\n"
    "v_mfma_f32_16x16x32_bf16 v[56:59], v[96:99], v[208:211], v[56:59]\n"
    "v_exp_f32 v78, v78\n"
    "v_mfma_f32_16x16x32_bf16 v[56:59], v[100:103], v[212:215], v[56:59]\n"
    "v_exp_f32 v79, v79\n"
    "v_mfma_f32_16x16x32_bf16 v[56:59], v[104:107], v[216:219], v[56:59]\n"
    "ds_read_b64_tr_b16 v[88:89], v122 offset:1024\n"
    "v_add_f32 v119, v119, v78\n"
    "v_mfma_f32_16x16x32_bf16 v[56:59], v[108:111], v[220:223], v[56:59]\n"
    "ds_read_b64_tr_b16 v[90:91], v122 offset:9216\n"
    "v_add_f32 v119, v119, v79\n"
    "v_mfma_f32_16x16x32_bf16 v[60:63], v[108:111], v[252:255], v[60:63]\n"
    "ds_read_b64_tr_b16 v[92:93], v122 offset:17408\n"
    "v_cvt_pk_bf16_f32 v47, v78, v79\n"
    "v_mfma_f32_16x16x32_bf16 v[60:63], v[104:107], v[248:251], v[60:63]\n"
    "ds_read_b64_tr_b16 v[94:95], v122 offset:25600\n"
    "v_mfma_f32_16x16x32_bf16 v[60:63], v[100:103], v[244:247], v[60:63]\n"
    "buffer_load_dwordx4 v123, s[40:43], s58 offen offset:2048 nt lds\n"
    "v_mfma_f32_16x16x32_bf16 v[60:63], v[96:99], v[240:243], v[60:63]\n"
    "s_waitcnt lgkmcnt(4)\n"
    "v_mfma_f32_16x16x32_bf16 a[48:51], v[80:83], v[0:3], a[48:51]\n"
    "v_exp_f32 v48, v48\n"
    "v_mfma_f32_16x16x32_bf16 a[48:51], v[84:87], v[16:19], a[48:51]\n"
    "v_exp_f32 v49, v49\n"
    "v_mfma_f32_16x16x32_bf16 a[52:55], v[84:87], v[20:23], a[52:55]\n"
    "ds_read_b64_tr_b16 v[96:97], v122 offset:2048\n"
    "v_add_f32 v116, v116, v48\n"
    "v_mfma_f32_16x16x32_bf16 a[52:55], v[80:83], v[4:7], a[52:55]\n"
    "ds_read_b64_tr_b16 v[98:99], v122 offset:10240\n"
    "v_add_f32 v116, v116, v49\n"
    "v_mfma_f32_16x16x32_bf16 a[56:59], v[80:83], v[8:11], a[56:59]\n"
    "ds_read_b64_tr_b16 v[100:101], v122 offset:18432\n"
    "v_cvt_pk_bf16_f32 v48, v48, v49\n"
    "v_mfma_f32_16x16x32_bf16 a[56:59], v[84:87], v[24:27], a[56:59]\n"
    "ds_read_b64_tr_b16 v[102:103], v122 offset:26624\n"
    "v_mfma_f32_16x16x32_bf16 a[60:63], v[84:87], v[28:31], a[60:63]\n"
    "v_mfma_f32_16x16x32_bf16 a[60:63], v[80:83], v[12:15], a[60:63]\n"
    "s_waitcnt lgkmcnt(4)\n"
    "v_mfma_f32_16x16x32_bf16 a[112:115], v[88:91], v[0:3], a[112:115]\n"
    "v_exp_f32 v50, v50\n"
    "v_mfma_f32_16x16x32_bf16 a[112:115], v[92:95], v[16:19], a[112:115]\n"
    "v_exp_f32 v51, v51\n"
    "v_mfma_f32_16x16x32_bf16 a[116:119], v[92:95], v[20:23], a[116:119]\n"
    "ds_read_b64_tr_b16 v[104:105], v122 offset:3072\n"
    "v_add_f32 v116, v116, v50\n"
    "v_mfma_f32_16x16x32_bf16 a[116:119], v[88:91], v[4:7], a[116:119]\n"
    "ds_read_b64_tr_b16 v[106:107], v122 offset:11264\n"
    "v_add_f32 v116, v116, v51\n"
    "v_mfma_f32_16x16x32_bf16 a[120:123], v[88:91], v[8:11], a[120:123]\n"
    "ds_read_b64_tr_b16 v[108:109], v122 offset:19456\n"
    "v_cvt_pk_bf16_f32 v49, v50, v51\n"
    "v_mfma_f32_16x16x32_bf16 a[120:123], v[92:95], v[24:27], a[120:123]\n"
    "ds_read_b64_tr_b16 v[110:111], v122 offset:27648\n"
    "v_mfma_f32_16x16x32_bf16 a[124:127], v[92:95], v[28:31], a[124:127]\n"
    "buffer_load_dwordx4 v123, s[40:43], s59 offen offset:3072 nt lds\n"
    "v_mfma_f32_16x16x32_bf16 a[124:127], v[88:91], v[12:15], a[124:127]\n"
    "s_waitcnt lgkmcnt(4)\n"
    "v_mfma_f32_16x16x32_bf16 a[176:179], v[96:99], v[0:3], a[176:179]\n"
    "v_exp_f32 v52, v52\n"
    "v_mfma_f32_16x16x32_bf16 a[176:179], v[100:103], v[16:19], a[176:179]\n"
    "v_exp_f32 v53, v53\n"
    "v_mfma_f32_16x16x32_bf16 a[180:183], v[100:103], v[20:23], a[180:183]\n"
    "ds_read_b128 v[80:83], v120 offset:57344\n"
    "v_add_f32 v117, v117, v52\n"
    "v_mfma_f32_16x16x32_bf16 a[180:183], v[96:99], v[4:7], a[180:183]\n"
    "ds_read_b128 v[84:87], v121 offset:57344\n"
    "v_add_f32 v117, v117, v53\n"
    "v_mfma_f32_16x16x32_bf16 a[184:187], v[96:99], v[8:11], a[184:187]\n"
    "ds_read_b128 v[88:91], v120 offset:58368\n"
    "v_cvt_pk_bf16_f32 v52, v52, v53\n"
    "v_mfma_f32_16x16x32_bf16 a[184:187], v[100:103], v[24:27], a[184:187]\n"
    "ds_read_b128 v[92:95], v121 offset:58368\n"
    "v_mfma_f32_16x16x32_bf16 a[188:191], v[100:103], v[28:31], a[188:191]\n"
    "v_mfma_f32_16x16x32_bf16 a[188:191], v[96:99], v[12:15], a[188:191]\n"
    "s_waitcnt lgkmcnt(4)\n"
    "v_mfma_f32_16x16x32_bf16 a[240:243], v[104:107], v[0:3], a[240:243]\n"
    "v_exp_f32 v54, v54\n"
    "v_mfma_f32_16x16x32_bf16 a[240:243], v[108:111], v[16:19], a[240:243]\n"
    "v_exp_f32 v55, v55\n"
    "v_mfma_f32_16x16x32_bf16 a[244:247], v[108:111], v[20:23], a[244:247]\n"
    "ds_read_b128 v[96:99], v120 offset:59392\n"
    "v_add_f32 v117, v117, v54\n"
    "v_mfma_f32_16x16x32_bf16 a[244:247], v[104:107], v[4:7], a[244:247]\n"
    "ds_read_b128 v[100:103], v121 offset:59392\n"
    "v_add_f32 v117, v117, v55\n"
    "v_mfma_f32_16x16x32_bf16 a[248:251], v[104:107], v[8:11], a[248:251]\n"
    "v_cvt_pk_bf16_f32 v53, v54, v55\n"
    "v_mfma_f32_16x16x32_bf16 a[248:251], v[108:111], v[24:27], a[248:251]\n"
    "v_mfma_f32_16x16x32_bf16 a[252:255], v[108:111], v[28:31], a[252:255]\n"
    "v_mfma_f32_16x16x32_bf16 a[252:255], v[104:107], v[12:15], a[252:255]\n"
    "s_waitcnt lgkmcnt(2)\n"
    "v_mfma_f32_16x16x32_bf16 v[64:67], v[80:83], v[128:131], v[112:115]\n"
    "v_exp_f32 v56, v56\n"
    "v_mfma_f32_16x16x32_bf16 v[64:67], v[84:87], v[132:135], v[64:67]\n"
    "v_exp_f32 v57, v57\n"
    "v_mfma_f32_16x16x32_bf16 v[64:67], v[88:91], v[136:139], v[64:67]\n"
    "ds_read_b128 v[104:107], v120 offset:60416\n"
    "v_add_f32 v118, v118, v56\n"
    "v_mfma_f32_16x16x32_bf16 v[64:67], v[92:95], v[140:143], v[64:67]\n"
    "ds_read_b128 v[108:111], v121 offset:60416\n"
    "v_add_f32 v118, v118, v57\n"
    "v_mfma_f32_16x16x32_bf16 v[68:71], v[92:95], v[172:175], v[112:115]\n"
    "v_cvt_pk_bf16_f32 v56, v56, v57\n"
    "s_add_u32 m0, s62, 0x4000\n"
    "v_mfma_f32_16x16x32_bf16 v[68:71], v[88:91], v[168:171], v[68:71]\n"
    "buffer_load_dwordx4 v123, s[44:47], s56 offen offset:0 nt lds\n"
    "v_mfma_f32_16x16x32_bf16 v[68:71], v[84:87], v[164:167], v[68:71]\n"
    "v_xor_b32 v122, 32, v122\n"
    "v_mfma_f32_16x16x32_bf16 v[68:71], v[80:83], v[160:163], v[68:71]\n"
    "v_mfma_f32_16x16x32_bf16 v[72:75], v[80:83], v[192:195], v[112:115]\n"
    "v_exp_f32 v58, v58\n"
    "v_mfma_f32_16x16x32_bf16 v[72:75], v[84:87], v[196:199], v[72:75]\n"
    "v_exp_f32 v59, v59\n"
    "v_mfma_f32_16x16x32_bf16 v[72:75], v[88:91], v[200:203], v[72:75]\n"
    "v_add_f32 v118, v118, v58\n"
    "v_mfma_f32_16x16x32_bf16 v[72:75], v[92:95], v[204:207], v[72:75]\n"
    "v_add_f32 v118, v118, v59\n"
    "v_mfma_f32_16x16x32_bf16 v[76:79], v[92:95], v[236:239], v[112:115]\n"
    "v_cvt_pk_bf16_f32 v57, v58, v59\n"
    "v_mfma_f32_16x16x32_bf16 v[76:79], v[88:91], v[232:235], v[76:79]\n"
    "v_mfma_f32_16x16x32_bf16 v[76:79], v[84:87], v[228:231], v[76:79]\n"
    "v_mfma_f32_16x16x32_bf16 v[76:79], v[80:83], v[224:227], v[76:79]\n"
    "s_waitcnt lgkmcnt(0)\n"
    "v_mfma_f32_16x16x32_bf16 v[64:67], v[96:99], v[144:147], v[64:67]\n"
    "v_exp_f32 v60, v60\n"
    "v_mfma_f32_16x16x32_bf16 v[64:67], v[100:103], v[148:151], v[64:67]\n"
    "v_exp_f32 v61, v61\n"
    "v_mfma_f32_16x16x32_bf16 v[64:67], v[104:107], v[152:155], v[64:67]\n"
    "ds_read_b64_tr_b16 v[80:81], v122 offset:0\n"
    "v_add_f32 v119, v119, v60\n"
    "v_mfma_f32_16x16x32_bf16 v[64:67], v[108:111], v[156:159], v[64:67]\n"
    "ds_read_b64_tr_b16 v[82:83], v122 offset:8192\n"
    "v_add_f32 v119, v119, v61\n"
    "v_mfma_f32_16x16x32_bf16 v[68:71], v[108:111], v[188:191], v[68:71]\n"
    "ds_read_b64_tr_b16 v[84:85], v122 offset:16384\n"
    "v_cvt_pk_bf16_f32 v60, v60, v61\n"
    "v_mfma_f32_16x16x32_bf16 v[68:71], v[104:107], v[184:187], v[68:71]\n"
    "ds_read_b64_tr_b16 v[86:87], v122 offset:24576\n"
    "s_add_u32 s62, s63, s61\n"
    "v_mfma_f32_16x16x32_bf16 v[68:71], v[100:103], v[180:183], v[68:71]\n"
    "buffer_load_dwordx4 v123, s[44:47], s57 offen offset:1024 nt lds\n"
    "v_mfma_f32_16x16x32_bf16 v[68:71], v[96:99], v[176:179], v[68:71]\n"
    "s_add_u32 s63, s63, 0x8000\n"
    "s_cmp_eq_u32 s63, 0x18000\n"
    "v_mfma_f32_16x16x32_bf16 v[72:75], v[96:99], v[208:211], v[72:75]\n"
    "v_exp_f32 v62, v62\n"
    "v_mfma_f32_16x16x32_bf16 v[72:75], v[100:103], v[212:215], v[72:75]\n"
    "v_exp_f32 v63, v63\n"
    "v_mfma_f32_16x16x32_bf16 v[72:75], v[104:107], v[216:219], v[72:75]\n"
    "ds_read_b64_tr_b16 v[88:89], v122 offset:1024\n"
    "v_add_f32 v119, v119, v62\n"
    "v_mfma_f32_16x16x32_bf16 v[72:75], v[108:111], v[220:223], v[72:75]\n"
    "ds_read_b64_tr_b16 v[90:91], v122 offset:9216\n"
    "v_add_f32 v119, v119, v63\n"
    "v_mfma_f32_16x16x32_bf16 v[76:79], v[108:111], v[252:255], v[76:79]\n"
    "ds_read_b64_tr_b16 v[92:93], v122 offset:17408\n"
    "v_cvt_pk_bf16_f32 v61, v62, v63\n"
    "v_mfma_f32_16x16x32_bf16 v[76:79], v[104:107], v[248:251], v[76:79]\n"
    "ds_read_b64_tr_b16 v[94:95], v122 offset:25600\n"
    "s_cselect_b32 s64, s68, s67\n"
    "v_mfma_f32_16x16x32_bf16 v[76:79], v[100:103], v[244:247], v[76:79]\n"
    "buffer_load_dwordx4 v123, s[44:47], s58 offen offset:2048 nt lds\n"
    "v_mfma_f32_16x16x32_bf16 v[76:79], v[96:99], v[240:243], v[76:79]\n"
    "s_cselect_b32 s63, 0, s63\n"
    "s_waitcnt lgkmcnt(4)\n"
    "v_mfma_f32_16x16x32_bf16 a[32:35], v[80:83], v[0:3], a[32:35]\n"
    "v_exp_f32 v64, v64\n"
    "v_mfma_f32_16x16x32_bf16 a[32:35], v[84:87], v[16:19], a[32:35]\n"
    "v_exp_f32 v65, v65\n"
    "v_mfma_f32_16x16x32_bf16 a[36:39], v[84:87], v[20:23], a[36:39]\n"
    "ds_read_b64_tr_b16 v[96:97], v122 offset:2048\n"
    "v_add_f32 v116, v116, v64\n"
    "v_mfma_f32_16x16x32_bf16 a[36:39], v[80:83], v[4:7], a[36:39]\n"
    "ds_read_b64_tr_b16 v[98:99], v122 offset:10240\n"
    "v_add_f32 v116, v116, v65\n"
    "v_mfma_f32_16x16x32_bf16 a[40:43], v[80:83], v[8:11], a[40:43]\n"
    "ds_read_b64_tr_b16 v[100:101], v122 offset:18432\n"
    "v_cvt_pk_bf16_f32 v50, v64, v65\n"
    "v_mfma_f32_16x16x32_bf16 a[40:43], v[84:87], v[24:27], a[40:43]\n"
    "ds_read_b64_tr_b16 v[102:103], v122 offset:26624\n"
    "v_mfma_f32_16x16x32_bf16 a[44:47], v[84:87], v[28:31], a[44:47]\n"
    "v_mfma_f32_16x16x32_bf16 a[44:47], v[80:83], v[12:15], a[44:47]\n"
    "s_waitcnt lgkmcnt(4)\n"
    "v_mfma_f32_16x16x32_bf16 a[96:99], v[88:91], v[0:3], a[96:99]\n"
    "v_exp_f32 v66, v66\n"
    "v_mfma_f32_16x16x32_bf16 a[96:99], v[92:95], v[16:19], a[96:99]\n"
    "v_exp_f32 v67, v67\n"
    "v_mfma_f32_16x16x32_bf16 a[100:103], v[92:95], v[20:23], a[100:103]\n"
    "ds_read_b64_tr_b16 v[104:105], v122 offset:3072\n"
    "v_add_f32 v116, v116, v66\n"
    "v_mfma_f32_16x16x32_bf16 a[100:103], v[88:91], v[4:7], a[100:103]\n"
    "ds_read_b64_tr_b16 v[106:107], v122 offset:11264\n"
    "v_add_f32 v116, v116, v67\n"
    "v_mfma_f32_16x16x32_bf16 a[104:107], v[88:91], v[8:11], a[104:107]\n"
    "ds_read_b64_tr_b16 v[108:109], v122 offset:19456\n"
    "v_cvt_pk_bf16_f32 v51, v66, v67\n"
    "v_mfma_f32_16x16x32_bf16 a[104:107], v[92:95], v[24:27], a[104:107]\n"
    "ds_read_b64_tr_b16 v[110:111], v122 offset:27648\n"
    "v_mfma_f32_16x16x32_bf16 a[108:111], v[92:95], v[28:31], a[108:111]\n"
    "buffer_load_dwordx4 v123, s[44:47], s59 offen offset:3072 nt lds\n"
    "v_mfma_f32_16x16x32_bf16 a[108:111], v[88:91], v[12:15], a[108:111]\n"
    "s_waitcnt lgkmcnt(4)\n"
    "v_mfma_f32_16x16x32_bf16 a[160:163], v[96:99], v[0:3], a[160:163]\n"
    "v_exp_f32 v68, v68\n"
    "v_mfma_f32_16x16x32_bf16 a[160:163], v[100:103], v[16:19], a[160:163]\n"
    "v_exp_f32 v69, v69\n"
    "v_mfma_f32_16x16x32_bf16 a[164:167], v[100:103], v[20:23], a[164:167]\n"
    "ds_read_b128 v[80:83], v120 offset:0\n"
    "v_add_f32 v117, v117, v68\n"
    "v_mfma_f32_16x16x32_bf16 a[164:167], v[96:99], v[4:7], a[164:167]\n"
    "ds_read_b128 v[84:87], v121 offset:0\n"
    "v_add_f32 v117, v117, v69\n"
    "v_mfma_f32_16x16x32_bf16 a[168:171], v[96:99], v[8:11], a[168:171]\n"
    "ds_read_b128 v[88:91], v120 offset:1024\n"
    "v_cvt_pk_bf16_f32 v54, v68, v69\n"
    "v_mfma_f32_16x16x32_bf16 a[168:171], v[100:103], v[24:27], a[168:171]\n"
    "ds_read_b128 v[92:95], v121 offset:1024\n"
    "v_mfma_f32_16x16x32_bf16 a[172:175], v[100:103], v[28:31], a[172:175]\n"
    "v_mfma_f32_16x16x32_bf16 a[172:175], v[96:99], v[12:15], a[172:175]\n"
    "s_waitcnt lgkmcnt(4)\n"
    "v_mfma_f32_16x16x32_bf16 a[224:227], v[104:107], v[0:3], a[224:227]\n"
    "v_exp_f32 v70, v70\n"
    "v_mfma_f32_16x16x32_bf16 a[224:227], v[108:111], v[16:19], a[224:227]\n"
    "v_exp_f32 v71, v71\n"
    "v_mfma_f32_16x16x32_bf16 a[228:231], v[108:111], v[20:23], a[228:231]\n"
    "ds_read_b128 v[96:99], v120 offset:2048\n"
    "v_add_f32 v117, v117, v70\n"
    "v_mfma_f32_16x16x32_bf16 a[228:231], v[104:107], v[4:7], a[228:231]\n"
    "ds_read_b128 v[100:103], v121 offset:2048\n"
    "v_add_f32 v117, v117, v71\n"
    "v_mfma_f32_16x16x32_bf16 a[232:235], v[104:107], v[8:11], a[232:235]\n"
    "v_cvt_pk_bf16_f32 v55, v70, v71\n"
    "v_mfma_f32_16x16x32_bf16 a[232:235], v[108:111], v[24:27], a[232:235]\n"
    "s_sub_u32 s65, s65, 1\n"
    "s_cmp_lg_u32 s65, 0\n"
    "v_mfma_f32_16x16x32_bf16 a[236:239], v[108:111], v[28:31], a[236:239]\n"
    "s_waitcnt vmcnt(12) lgkmcnt(2)\n"
    "v_mfma_f32_16x16x32_bf16 a[236:239], v[104:107], v[12:15], a[236:239]\n"
    "s_barrier\n"
    "s_cbranch_scc1 LBODY%=\n"
    "LTAIL%=:\n"
    "v_mfma_f32_16x16x32_bf16 v[0:3], v[80:83], v[128:131], v[112:115]\n"
    "v_exp_f32 v72, v72\n"
    "v_mfma_f32_16x16x32_bf16 v[0:3], v[84:87], v[132:135], v[0:3]\n"
    "v_exp_f32 v73, v73\n"
    "v_mfma_f32_16x16x32_bf16 v[0:3], v[88:91], v[136:139], v[0:3]\n"
    "ds_read_b128 v[104:107], v120 offset:3072\n"
    "v_add_f32 v118, v118, v72\n"
    "v_mfma_f32_16x16x32_bf16 v[0:3], v[92:95], v[140:143], v[0:3]\n"
    "ds_read_b128 v[108:111], v121 offset:3072\n"
    "v_add_f32 v118, v118, v73\n"
    "v_mfma_f32_16x16x32_bf16 v[4:7], v[92:95], v[172:175], v[112:115]\n"
    "v_cvt_pk_bf16_f32 v58, v72, v73\n"
    "v_mbcnt_lo_u32_b32 v127, -1, 0\n"
    "v_mfma_f32_16x16x32_bf16 v[4:7], v[88:91], v[168:171], v[4:7]\n"
    "v_mbcnt_hi_u32_b32 v127, -1, v127\n"
    "s_lshl_b32 s69, s20, 13\n"
    "v_mfma_f32_16x16x32_bf16 v[4:7], v[84:87], v[164:167], v[4:7]\n"
    "s_add_u32 s69, s69, s22\n"
    "s_add_u32 s69, s69, 0x10000\n"
    "v_mfma_f32_16x16x32_bf16 v[4:7], v[80:83], v[160:163], v[4:7]\n"
    "v_lshl_add_u32 v127, v127, 4, s69\n"
    "v_mfma_f32_16x16x32_bf16 v[8:11], v[80:83], v[192:195], v[112:115]\n"
    "v_exp_f32 v74, v74\n"
    "v_mfma_f32_16x16x32_bf16 v[8:11], v[84:87], v[196:199], v[8:11]\n"
    "v_exp_f32 v75, v75\n"
    "v_mfma_f32_16x16x32_bf16 v[8:11], v[88:91], v[200:203], v[8:11]\n"
    "ds_write_b128 v127, v[32:35] offset:0\n"
    "v_add_f32 v118, v118, v74\n"
    "v_mfma_f32_16x16x32_bf16 v[8:11], v[92:95], v[204:207], v[8:11]\n"
    "v_add_f32 v118, v118, v75\n"
    "v_add_u32 v122, 0x8000, v122\n"
    "v_mfma_f32_16x16x32_bf16 v[12:15], v[92:95], v[236:239], v[112:115]\n"
    "v_cvt_pk_bf16_f32 v59, v74, v75\n"
    "v_xor_b32 v124, 0x40, v122\n"
    "v_mfma_f32_16x16x32_bf16 v[12:15], v[88:91], v[232:235], v[12:15]\n"
    "v_xor_b32 v125, 0x60, v122\n"
    "v_mov_b32 v126, v122\n"
    "v_mfma_f32_16x16x32_bf16 v[12:15], v[84:87], v[228:231], v[12:15]\n"
    "ds_write_b128 v127, v[36:39] offset:1024\n"
    "v_mfma_f32_16x16x32_bf16 v[12:15], v[80:83], v[224:227], v[12:15]\n"
    "v_xor_b32 v122, 0x20, v122\n"
    "s_waitcnt lgkmcnt(2)\n"
    "v_mfma_f32_16x16x32_bf16 v[0:3], v[96:99], v[144:147], v[0:3]\n"
    "v_exp_f32 v76, v76\n"
    "v_mfma_f32_16x16x32_bf16 v[0:3], v[100:103], v[148:151], v[0:3]\n"
    "v_exp_f32 v77, v77\n"
    "v_mfma_f32_16x16x32_bf16 v[0:3], v[104:107], v[152:155], v[0:3]\n"
    "ds_read_b128 v[80:83], v120 offset:8192\n"
    "v_add_f32 v119, v119, v76\n"
    "v_mfma_f32_16x16x32_bf16 v[0:3], v[108:111], v[156:159], v[0:3]\n"
    "ds_read_b128 v[84:87], v121 offset:8192\n"
    "v_add_f32 v119, v119, v77\n"
    "v_mfma_f32_16x16x32_bf16 v[4:7], v[108:111], v[188:191], v[4:7]\n"
    "ds_read_b128 v[88:91], v120 offset:9216\n"
    "v_cvt_pk_bf16_f32 v62, v76, v77\n"
    "v_mfma_f32_16x16x32_bf16 v[4:7], v[104:107], v[184:187], v[4:7]\n"
    "ds_read_b128 v[92:95], v121 offset:9216\n"
    "v_mfma_f32_16x16x32_bf16 v[4:7], v[100:103], v[180:183], v[4:7]\n"
    "v_mfma_f32_16x16x32_bf16 v[4:7], v[96:99], v[176:179], v[4:7]\n"
    "ds_write_b128 v127, v[40:43] offset:2048\n"
    "v_mfma_f32_16x16x32_bf16 v[8:11], v[96:99], v[208:211], v[8:11]\n"
    "v_exp_f32 v78, v78\n"
    "v_mfma_f32_16x16x32_bf16 v[8:11], v[100:103], v[212:215], v[8:11]\n"
    "v_exp_f32 v79, v79\n"
    "v_mfma_f32_16x16x32_bf16 v[8:11], v[104:107], v[216:219], v[8:11]\n"
    "v_add_f32 v119, v119, v78\n"
    "v_mfma_f32_16x16x32_bf16 v[8:11], v[108:111], v[220:223], v[8:11]\n"
    "ds_write_b128 v127, v[44:47] offset:3072\n"
    "v_add_f32 v119, v119, v79\n"
    "v_mfma_f32_16x16x32_bf16 v[12:15], v[108:111], v[252:255], v[12:15]\n"
    "v_cvt_pk_bf16_f32 v63, v78, v79\n"
    "v_mfma_f32_16x16x32_bf16 v[12:15], v[104:107], v[248:251], v[12:15]\n"
    "v_mfma_f32_16x16x32_bf16 v[12:15], v[100:103], v[244:247], v[12:15]\n"
    "ds_write_b128 v127, v[48:51] offset:4096\n"
    "v_mfma_f32_16x16x32_bf16 v[12:15], v[96:99], v[240:243], v[12:15]\n"
    "s_waitcnt lgkmcnt(3)\n"
    "v_mfma_f32_16x16x32_bf16 v[64:67], v[80:83], v[128:131], v[112:115]\n"
    "v_exp_f32 v0, v0\n"
    "v_add_u32 v123, 0x4000, v123\n"
    "v_mfma_f32_16x16x32_bf16 v[64:67], v[84:87], v[132:135], v[64:67]\n"
    "v_exp_f32 v1, v1\n"
    "s_add_u32 m0, s60, 0xc000\n"
    "v_mfma_f32_16x16x32_bf16 v[64:67], v[88:91], v[136:139], v[64:67]\n"
    "ds_read_b128 v[96:99], v120 offset:10240\n"
    "v_add_f32 v116, v116, v0\n"
    "v_mfma_f32_16x16x32_bf16 v[64:67], v[92:95], v[140:143], v[64:67]\n"
    "ds_read_b128 v[100:103], v121 offset:10240\n"
    "v_add_f32 v116, v116, v1\n"
    "v_mfma_f32_16x16x32_bf16 v[68:71], v[92:95], v[172:175], v[112:115]\n"
    "buffer_load_dwordx4 v123, s[40:43], s56 offen offset:0 nt lds\n"
    "v_exp_f32 v2, v2\n"
    "v_mfma_f32_16x16x32_bf16 v[68:71], v[88:91], v[168:171], v[68:71]\n"
    "v_exp_f32 v3, v3\n"
    "v_mfma_f32_16x16x32_bf16 v[68:71], v[84:87], v[164:167], v[68:71]\n"
    "ds_read_b128 v[104:107], v120 offset:11264\n"
    "v_cvt_pk_bf16_f32 v0, v0, v1\n"
    "v_mfma_f32_16x16x32_bf16 v[68:71], v[80:83], v[160:163], v[68:71]\n"
    "ds_read_b128 v[108:111], v121 offset:11264\n"
    "v_add_f32 v116, v116, v2\n"
    "v_mfma_f32_16x16x32_bf16 v[72:75], v[80:83], v[192:195], v[112:115]\n"
    "buffer_load_dwordx4 v123, s[40:43], s57 offen offset:1024 nt lds\n"
    "v_exp_f32 v4, v4\n"
    "v_mfma_f32_16x16x32_bf16 v[72:75], v[84:87], v[196:199], v[72:75]\n"
    "v_exp_f32 v5, v5\n"
    "v_mfma_f32_16x16x32_bf16 v[72:75], v[88:91], v[200:203], v[72:75]\n"
    "v_add_f32 v116, v116, v3\n"
    "v_cvt_pk_bf16_f32 v1, v2, v3\n"
    "v_mfma_f32_16x16x32_bf16 v[72:75], v[92:95], v[204:207], v[72:75]\n"
    "v_exp_f32 v6, v6\n"
    "v_mfma_f32_16x16x32_bf16 v[76:79], v[92:95], v[236:239], v[112:115]\n"
    "v_exp_f32 v7, v7\n"
    "v_mfma_f32_16x16x32_bf16 v[76:79], v[88:91], v[232:235], v[76:79]\n"
    "ds_write_b128 v127, v[52:55] offset:5120\n"
    "v_add_f32 v117, v117, v4\n"
    "v_add_f32 v117, v117, v5\n"
    "v_mfma_f32_16x16x32_bf16 v[76:79], v[84:87], v[228:231], v[76:79]\n"
    "buffer_load_dwordx4 v123, s[40:43], s58 offen offset:2048 nt lds\n"
    "v_cvt_pk_bf16_f32 v4, v4, v5\n"
    "v_add_f32 v117, v117, v6\n"
    "v_mfma_f32_16x16x32_bf16 v[76:79], v[80:83], v[224:227], v[76:79]\n"
    "v_add_f32 v117, v117, v7\n"
    "v_cvt_pk_bf16_f32 v5, v6, v7\n"
    "s_waitcnt lgkmcnt(1)\n"
    "v_mfma_f32_16x16x32_bf16 v[64:67], v[96:99], v[144:147], v[64:67]\n"
    "v_exp_f32 v8, v8\n"
    "v_mfma_f32_16x16x32_bf16 v[64:67], v[100:103], v[148:151], v[64:67]\n"
    "v_exp_f32 v9, v9\n"
    "v_mfma_f32_16x16x32_bf16 v[64:67], v[104:107], v[152:155], v[64:67]\n"
    "ds_read_b128 v[80:83], v120 offset:16384\n"
    "v_add_f32 v118, v118, v8\n"
    "v_mfma_f32_16x16x32_bf16 v[64:67], v[108:111], v[156:159], v[64:67]\n"
    "ds_read_b128 v[84:87], v121 offset:16384\n"
    "v_add_f32 v118, v118, v9\n"
    "v_mfma_f32_16x16x32_bf16 v[68:71], v[108:111], v[188:191], v[68:71]\n"
    "v_exp_f32 v10, v10\n"
    "v_mfma_f32_16x16x32_bf16 v[68:71], v[104:107], v[184:187], v[68:71]\n"
    "buffer_load_dwordx4 v123, s[40:43], s59 offen offset:3072 nt lds\n"
    "v_exp_f32 v11, v11\n"
    "v_mfma_f32_16x16x32_bf16 v[68:71], v[100:103], v[180:183], v[68:71]\n"
    "ds_read_b128 v[88:91], v120 offset:17408\n"
    "v_cvt_pk_bf16_f32 v8, v8, v9\n"
    "v_mfma_f32_16x16x32_bf16 v[68:71], v[96:99], v[176:179], v[68:71]\n"
    "ds_read_b128 v[92:95], v121 offset:17408\n"
    "v_add_f32 v118, v118, v10\n"
    "v_mfma_f32_16x16x32_bf16 v[72:75], v[96:99], v[208:211], v[72:75]\n"
    "v_exp_f32 v12, v12\n"
    "v_mfma_f32_16x16x32_bf16 v[72:75], v[100:103], v[212:215], v[72:75]\n"
    "v_exp_f32 v13, v13\n"
    "v_mfma_f32_16x16x32_bf16 v[72:75], v[104:107], v[216:219], v[72:75]\n"
    "v_add_f32 v118, v118, v11\n"
    "v_cvt_pk_bf16_f32 v9, v10, v11\n"
    "v_mfma_f32_16x16x32_bf16 v[72:75], v[108:111], v[220:223], v[72:75]\n"
    "v_exp_f32 v14, v14\n"
    "v_mfma_f32_16x16x32_bf16 v[76:79], v[108:111], v[252:255], v[76:79]\n"
    "v_exp_f32 v15, v15\n"
    "v_mfma_f32_16x16x32_bf16 v[76:79], v[104:107], v[248:251], v[76:79]\n"
    "ds_write_b128 v127, v[56:59] offset:6144\n"
    "v_add_f32 v119, v119, v12\n"
    "v_add_f32 v119, v119, v13\n"
    "v_mfma_f32_16x16x32_bf16 v[76:79], v[100:103], v[244:247], v[76:79]\n"
    "v_cvt_pk_bf16_f32 v12, v12, v13\n"
    "v_add_f32 v119, v119, v14\n"
    "v_mfma_f32_16x16x32_bf16 v[76:79], v[96:99], v[240:243], v[76:79]\n"
    "v_add_f32 v119, v119, v15\n"
    "v_cvt_pk_bf16_f32 v13, v14, v15\n"
    "s_waitcnt vmcnt(8) lgkmcnt(1)\n"
    "s_barrier\n"
    "v_mfma_f32_16x16x32_bf16 v[16:19], v[80:83], v[128:131], v[112:115]\n"
    "v_exp_f32 v64, v64\n"
    "s_add_u32 m0, s60, 0x0\n"
    "v_mfma_f32_16x16x32_bf16 v[16:19], v[84:87], v[132:135], v[16:19]\n"
    "buffer_load_dwordx4 v123, s[44:47], s56 offen offset:0 nt lds\n"
    "v_exp_f32 v65, v65\n"
    "v_mfma_f32_16x16x32_bf16 v[16:19], v[88:91], v[136:139], v[16:19]\n"
    "ds_read_b128 v[96:99], v120 offset:18432\n"
    "v_add_f32 v116, v116, v64\n"
    "v_mfma_f32_16x16x32_bf16 v[16:19], v[92:95], v[140:143], v[16:19]\n"
    "ds_read_b128 v[100:103], v121 offset:18432\n"
    "v_add_f32 v116, v116, v65\n"
    "v_mfma_f32_16x16x32_bf16 v[20:23], v[92:95], v[172:175], v[112:115]\n"
    "v_exp_f32 v66, v66\n"
    "v_mfma_f32_16x16x32_bf16 v[20:23], v[88:91], v[168:171], v[20:23]\n"
    "v_exp_f32 v67, v67\n"
    "v_mfma_f32_16x16x32_bf16 v[20:23], v[84:87], v[164:167], v[20:23]\n"
    "ds_read_b128 v[104:107], v120 offset:19456\n"
    "v_cvt_pk_bf16_f32 v2, v64, v65\n"
    "v_mfma_f32_16x16x32_bf16 v[20:23], v[80:83], v[160:163], v[20:23]\n"
    "ds_read_b128 v[108:111], v121 offset:19456\n"
    "v_add_f32 v116, v116, v66\n"
    "v_mfma_f32_16x16x32_bf16 v[24:27], v[80:83], v[192:195], v[112:115]\n"
    "v_exp_f32 v68, v68\n"
    "v_mfma_f32_16x16x32_bf16 v[24:27], v[84:87], v[196:199], v[24:27]\n"
    "v_exp_f32 v69, v69\n"
    "v_mfma_f32_16x16x32_bf16 v[24:27], v[88:91], v[200:203], v[24:27]\n"
    "v_add_f32 v116, v116, v67\n"
    "v_cvt_pk_bf16_f32 v3, v66, v67\n"
    "v_mfma_f32_16x16x32_bf16 v[24:27], v[92:95], v[204:207], v[24:27]\n"
    "v_exp_f32 v70, v70\n"
    "v_mfma_f32_16x16x32_bf16 v[28:31], v[92:95], v[236:239], v[112:115]\n"
    "v_exp_f32 v71, v71\n"
    "v_mfma_f32_16x16x32_bf16 v[28:31], v[88:91], v[232:235], v[28:31]\n"
    "ds_write_b128 v127, v[60:63] offset:7168\n"
    "v_add_f32 v117, v117, v68\n"
    "v_add_f32 v117, v117, v69\n"
    "v_mfma_f32_16x16x32_bf16 v[28:31], v[84:87], v[228:231], v[28:31]\n"
    "v_cvt_pk_bf16_f32 v6, v68, v69\n"
    "v_add_f32 v117, v117, v70\n"
    "v_mfma_f32_16x16x32_bf16 v[28:31], v[80:83], v[224:227], v[28:31]\n"
    "buffer_load_dwordx4 v123, s[44:47], s57 offen offset:1024 nt lds\n"
    "v_add_f32 v117, v117, v71\n"
    "v_cvt_pk_bf16_f32 v7, v70, v71\n"
    "s_waitcnt lgkmcnt(1)\n"
    "v_mfma_f32_16x16x32_bf16 v[16:19], v[96:99], v[144:147], v[16:19]\n"
    "v_exp_f32 v72, v72\n"
    "v_mfma_f32_16x16x32_bf16 v[16:19], v[100:103], v[148:151], v[16:19]\n"
    "v_exp_f32 v73, v73\n"
    "v_mfma_f32_16x16x32_bf16 v[16:19], v[104:107], v[152:155], v[16:19]\n"
    "ds_read_b128 v[80:83], v120 offset:24576\n"
    "v_add_f32 v118, v118, v72\n"
    "v_mfma_f32_16x16x32_bf16 v[16:19], v[108:111], v[156:159], v[16:19]\n"
    "ds_read_b128 v[84:87], v121 offset:24576\n"
    "v_add_f32 v118, v118, v73\n"
    "v_mfma_f32_16x16x32_bf16 v[20:23], v[108:111], v[188:191], v[20:23]\n"
    "v_exp_f32 v74, v74\n"
    "v_mfma_f32_16x16x32_bf16 v[20:23], v[104:107], v[184:187], v[20:23]\n"
    "v_exp_f32 v75, v75\n"
    "v_mfma_f32_16x16x32_bf16 v[20:23], v[100:103], v[180:183], v[20:23]\n"
    "ds_read_b128 v[88:91], v120 offset:25600\n"
    "v_cvt_pk_bf16_f32 v10, v72, v73\n"
    "v_mfma_f32_16x16x32_bf16 v[20:23], v[96:99], v[176:179], v[20:23]\n"
    "ds_read_b128 v[92:95], v121 offset:25600\n"
    "v_add_f32 v118, v118, v74\n"
    "v_mfma_f32_16x16x32_bf16 v[24:27], v[96:99], v[208:211], v[24:27]\n"
    "v_exp_f32 v76, v76\n"
    "v_mfma_f32_16x16x32_bf16 v[24:27], v[100:103], v[212:215], v[24:27]\n"
    "v_exp_f32 v77, v77\n"
    "v_mfma_f32_16x16x32_bf16 v[24:27], v[104:107], v[216:219], v[24:27]\n"
    "v_add_f32 v118, v118, v75\n"
    "v_cvt_pk_bf16_f32 v11, v74, v75\n"
    "v_mfma_f32_16x16x32_bf16 v[24:27], v[108:111], v[220:223], v[24:27]\n"
    "v_exp_f32 v78, v78\n"
    "v_mfma_f32_16x16x32_bf16 v[28:31], v[108:111], v[252:255], v[28:31]\n"
    "v_exp_f32 v79, v79\n"
    "v_mfma_f32_16x16x32_bf16 v[28:31], v[104:107], v[248:251], v[28:31]\n"
    "v_add_f32 v119, v119, v76\n"
    "v_add_f32 v119, v119, v77\n"
    "v_mfma_f32_16x16x32_bf16 v[28:31], v[100:103], v[244:247], v[28:31]\n"
    "buffer_load_dwordx4 v123, s[44:47], s58 offen offset:2048 nt lds\n"
    "v_cvt_pk_bf16_f32 v14, v76, v77\n"
    "v_add_f32 v119, v119, v78\n"
    "v_mfma_f32_16x16x32_bf16 v[28:31], v[96:99], v[240:243], v[28:31]\n"
    "v_add_f32 v119, v119, v79\n"
    "v_cvt_pk_bf16_f32 v15, v78, v79\n"
    "s_waitcnt lgkmcnt(0)\n"
    "v_mfma_f32_16x16x32_bf16 v[64:67], v[80:83], v[128:131], v[112:115]\n"
    "v_exp_f32 v16, v16\n"
    "v_mfma_f32_16x16x32_bf16 v[64:67], v[84:87], v[132:135], v[64:67]\n"
    "v_exp_f32 v17, v17\n"
    "v_mfma_f32_16x16x32_bf16 v[64:67], v[88:91], v[136:139], v[64:67]\n"
    "ds_read_b128 v[96:99], v120 offset:26624\n"
    "v_add_f32 v116, v116, v16\n"
    "v_mfma_f32_16x16x32_bf16 v[64:67], v[92:95], v[140:143], v[64:67]\n"
    "ds_read_b128 v[100:103], v121 offset:26624\n"
    "v_add_f32 v116, v116, v17\n"
    "v_mfma_f32_16x16x32_bf16 v[68:71], v[92:95], v[172:175], v[112:115]\n"
    "v_exp_f32 v18, v18\n"
    "v_mfma_f32_16x16x32_bf16 v[68:71], v[88:91], v[168:171], v[68:71]\n"
    "v_exp_f32 v19, v19\n"
    "v_mfma_f32_16x16x32_bf16 v[68:71], v[84:87], v[164:167], v[68:71]\n"
    "ds_read_b128 v[104:107], v120 offset:27648\n"
    "v_cvt_pk_bf16_f32 v16, v16, v17\n"
    "v_mfma_f32_16x16x32_bf16 v[68:71], v[80:83], v[160:163], v[68:71]\n"
    "ds_read_b128 v[108:111], v121 offset:27648\n"
    "v_add_f32 v116, v116, v18\n"
    "v_mfma_f32_16x16x32_bf16 v[72:75], v[80:83], v[192:195], v[112:115]\n"
    "v_exp_f32 v20, v20\n"
    "v_mfma_f32_16x16x32_bf16 v[72:75], v[84:87], v[196:199], v[72:75]\n"
    "v_exp_f32 v21, v21\n"
    "v_mfma_f32_16x16x32_bf16 v[72:75], v[88:91], v[200:203], v[72:75]\n"
    "v_add_f32 v116, v116, v19\n"
    "v_cvt_pk_bf16_f32 v17, v18, v19\n"
    "v_mfma_f32_16x16x32_bf16 v[72:75], v[92:95], v[204:207], v[72:75]\n"
    "v_exp_f32 v22, v22\n"
    "v_mfma_f32_16x16x32_bf16 v[76:79], v[92:95], v[236:239], v[112:115]\n"
    "v_exp_f32 v23, v23\n"
    "v_mfma_f32_16x16x32_bf16 v[76:79], v[88:91], v[232:235], v[76:79]\n"
    "buffer_load_dwordx4 v123, s[44:47], s59 offen offset:3072 nt lds\n"
    "v_add_f32 v117, v117, v20\n"
    "v_add_f32 v117, v117, v21\n"
    "v_mfma_f32_16x16x32_bf16 v[76:79], v[84:87], v[228:231], v[76:79]\n"
    "v_cvt_pk_bf16_f32 v20, v20, v21\n"
    "v_add_f32 v117, v117, v22\n"
    "v_mfma_f32_16x16x32_bf16 v[76:79], v[80:83], v[224:227], v[76:79]\n"
    "v_add_f32 v117, v117, v23\n"
    "v_cvt_pk_bf16_f32 v21, v22, v23\n"
    "s_waitcnt lgkmcnt(0)\n"
    "v_mfma_f32_16x16x32_bf16 v[64:67], v[96:99], v[144:147], v[64:67]\n"
    "v_exp_f32 v24, v24\n"
    "v_mfma_f32_16x16x32_bf16 v[64:67], v[100:103], v[148:151], v[64:67]\n"
    "v_exp_f32 v25, v25\n"
    "v_mfma_f32_16x16x32_bf16 v[64:67], v[104:107], v[152:155], v[64:67]\n"
    "ds_read_b128 v[80:83], v120 offset:32768\n"
    "v_add_f32 v118, v118, v24\n"
    "v_mfma_f32_16x16x32_bf16 v[64:67], v[108:111], v[156:159], v[64:67]\n"
    "ds_read_b128 v[84:87], v121 offset:32768\n"
    "v_add_f32 v118, v118, v25\n"
    "v_mfma_f32_16x16x32_bf16 v[68:71], v[108:111], v[188:191], v[68:71]\n"
    "v_exp_f32 v26, v26\n"
    "v_mfma_f32_16x16x32_bf16 v[68:71], v[104:107], v[184:187], v[68:71]\n"
    "v_exp_f32 v27, v27\n"
    "v_mfma_f32_16x16x32_bf16 v[68:71], v[100:103], v[180:183], v[68:71]\n"
    "ds_read_b128 v[88:91], v120 offset:33792\n"
    "v_cvt_pk_bf16_f32 v24, v24, v25\n"
    "v_mfma_f32_16x16x32_bf16 v[68:71], v[96:99], v[176:179], v[68:71]\n"
    "ds_read_b128 v[92:95], v121 offset:33792\n"
    "v_add_f32 v118, v118, v26\n"
    "v_mfma_f32_16x16x32_bf16 v[72:75], v[96:99], v[208:211], v[72:75]\n"
    "v_exp_f32 v28, v28\n"
    "v_mfma_f32_16x16x32_bf16 v[72:75], v[100:103], v[212:215], v[72:75]\n"
    "v_exp_f32 v29, v29\n"
    "v_mfma_f32_16x16x32_bf16 v[72:75], v[104:107], v[216:219], v[72:75]\n"
    "v_add_f32 v118, v118, v27\n"
    "v_cvt_pk_bf16_f32 v25, v26, v27\n"
    "v_mfma_f32_16x16x32_bf16 v[72:75], v[108:111], v[220:223], v[72:75]\n"
    "v_exp_f32 v30, v30\n"
    "v_mfma_f32_16x16x32_bf16 v[76:79], v[108:111], v[252:255], v[76:79]\n"
    "v_exp_f32 v31, v31\n"
    "v_mfma_f32_16x16x32_bf16 v[76:79], v[104:107], v[248:251], v[76:79]\n"
    "v_add_f32 v119, v119, v28\n"
    "v_add_f32 v119, v119, v29\n"
    "v_mfma_f32_16x16x32_bf16 v[76:79], v[100:103], v[244:247], v[76:79]\n"
    "v_cvt_pk_bf16_f32 v28, v28, v29\n"
    "v_add_f32 v119, v119, v30\n"
    "v_mfma_f32_16x16x32_bf16 v[76:79], v[96:99], v[240:243], v[76:79]\n"
    "v_add_f32 v119, v119, v31\n"
    "v_cvt_pk_bf16_f32 v29, v30, v31\n"
    "s_waitcnt vmcnt(4) lgkmcnt(0)\n"
    "s_barrier\n"
    "v_mfma_f32_16x16x32_bf16 v[32:35], v[80:83], v[128:131], v[112:115]\n"
    "v_exp_f32 v64, v64\n"
    "v_add_u32 v123, 0x4000, v123\n"
    "v_mfma_f32_16x16x32_bf16 v[32:35], v[84:87], v[132:135], v[32:35]\n"
    "v_exp_f32 v65, v65\n"
    "s_add_u32 m0, s60, 0x4000\n"
    "v_mfma_f32_16x16x32_bf16 v[32:35], v[88:91], v[136:139], v[32:35]\n"
    "ds_read_b128 v[96:99], v120 offset:34816\n"
    "v_add_f32 v116, v116, v64\n"
    "v_mfma_f32_16x16x32_bf16 v[32:35], v[92:95], v[140:143], v[32:35]\n"
    "ds_read_b128 v[100:103], v121 offset:34816\n"
    "v_add_f32 v116, v116, v65\n"
    "v_mfma_f32_16x16x32_bf16 v[36:39], v[92:95], v[172:175], v[112:115]\n"
    "buffer_load_dwordx4 v123, s[44:47], s56 offen offset:0 nt lds\n"
    "v_exp_f32 v66, v66\n"
    "v_mfma_f32_16x16x32_bf16 v[36:39], v[88:91], v[168:171], v[36:39]\n"
    "v_exp_f32 v67, v67\n"
    "v_mfma_f32_16x16x32_bf16 v[36:39], v[84:87], v[164:167], v[36:39]\n"
    "ds_read_b128 v[104:107], v120 offset:35840\n"
    "v_cvt_pk_bf16_f32 v18, v64, v65\n"
    "v_mfma_f32_16x16x32_bf16 v[36:39], v[80:83], v[160:163], v[36:39]\n"
    "ds_read_b128 v[108:111], v121 offset:35840\n"
    "v_add_f32 v116, v116, v66\n"
    "v_mfma_f32_16x16x32_bf16 v[40:43], v[80:83], v[192:195], v[112:115]\n"
    "buffer_load_dwordx4 v123, s[44:47], s57 offen offset:1024 nt lds\n"
    "v_exp_f32 v68, v68\n"
    "v_mfma_f32_16x16x32_bf16 v[40:43], v[84:87], v[196:199], v[40:43]\n"
    "v_exp_f32 v69, v69\n"
    "v_mfma_f32_16x16x32_bf16 v[40:43], v[88:91], v[200:203], v[40:43]\n"
    "v_add_f32 v116, v116, v67\n"
    "v_cvt_pk_bf16_f32 v19, v66, v67\n"
    "v_mfma_f32_16x16x32_bf16 v[40:43], v[92:95], v[204:207], v[40:43]\n"
    "v_exp_f32 v70, v70\n"
    "v_mfma_f32_16x16x32_bf16 v[44:47], v[92:95], v[236:239], v[112:115]\n"
    "v_exp_f32 v71, v71\n"
    "v_mfma_f32_16x16x32_bf16 v[44:47], v[88:91], v[232:235], v[44:47]\n"
    "v_add_f32 v117, v117, v68\n"
    "v_add_f32 v117, v117, v69\n"
    "v_mfma_f32_16x16x32_bf16 v[44:47], v[84:87], v[228:231], v[44:47]\n"
    "buffer_load_dwordx4 v123, s[44:47], s58 offen offset:2048 nt lds\n"
    "v_cvt_pk_bf16_f32 v22, v68, v69\n"
    "v_add_f32 v117, v117, v70\n"
    "v_mfma_f32_16x16x32_bf16 v[44:47], v[80:83], v[224:227], v[44:47]\n"
    "v_add_f32 v117, v117, v71\n"
    "v_cvt_pk_bf16_f32 v23, v70, v71\n"
    "s_waitcnt lgkmcnt(0)\n"
    "v_mfma_f32_16x16x32_bf16 v[32:35], v[96:99], v[144:147], v[32:35]\n"
    "v_exp_f32 v72, v72\n"
    "v_mfma_f32_16x16x32_bf16 v[32:35], v[100:103], v[148:151], v[32:35]\n"
    "v_exp_f32 v73, v73\n"
    "v_mfma_f32_16x16x32_bf16 v[32:35], v[104:107], v[152:155], v[32:35]\n"
    "ds_read_b128 v[80:83], v120 offset:40960\n"
    "v_add_f32 v118, v118, v72\n"
    "v_mfma_f32_16x16x32_bf16 v[32:35], v[108:111], v[156:159], v[32:35]\n"
    "ds_read_b128 v[84:87], v121 offset:40960\n"
    "v_add_f32 v118, v118, v73\n"
    "v_mfma_f32_16x16x32_bf16 v[36:39], v[108:111], v[188:191], v[36:39]\n"
    "v_exp_f32 v74, v74\n"
    "v_mfma_f32_16x16x32_bf16 v[36:39], v[104:107], v[184:187], v[36:39]\n"
    "buffer_load_dwordx4 v123, s[44:47], s59 offen offset:3072 nt lds\n"
    "v_exp_f32 v75, v75\n"
    "v_mfma_f32_16x16x32_bf16 v[36:39], v[100:103], v[180:183], v[36:39]\n"
    "ds_read_b128 v[88:91], v120 offset:41984\n"
    "v_cvt_pk_bf16_f32 v26, v72, v73\n"
    "v_mfma_f32_16x16x32_bf16 v[36:39], v[96:99], v[176:179], v[36:39]\n"
    "ds_read_b128 v[92:95], v121 offset:41984\n"
    "v_add_f32 v118, v118, v74\n"
    "v_mfma_f32_16x16x32_bf16 v[40:43], v[96:99], v[208:211], v[40:43]\n"
    "v_exp_f32 v76, v76\n"
    "v_mfma_f32_16x16x32_bf16 v[40:43], v[100:103], v[212:215], v[40:43]\n"
    "v_exp_f32 v77, v77\n"
    "v_mfma_f32_16x16x32_bf16 v[40:43], v[104:107], v[216:219], v[40:43]\n"
    "v_add_f32 v118, v118, v75\n"
    "v_cvt_pk_bf16_f32 v27, v74, v75\n"
    "v_mfma_f32_16x16x32_bf16 v[40:43], v[108:111], v[220:223], v[40:43]\n"
    "v_exp_f32 v78, v78\n"
    "v_mfma_f32_16x16x32_bf16 v[44:47], v[108:111], v[252:255], v[44:47]\n"
    "v_exp_f32 v79, v79\n"
    "v_mfma_f32_16x16x32_bf16 v[44:47], v[104:107], v[248:251], v[44:47]\n"
    "v_add_f32 v119, v119, v76\n"
    "v_add_f32 v119, v119, v77\n"
    "v_mfma_f32_16x16x32_bf16 v[44:47], v[100:103], v[244:247], v[44:47]\n"
    "v_cvt_pk_bf16_f32 v30, v76, v77\n"
    "v_add_f32 v119, v119, v78\n"
    "v_mfma_f32_16x16x32_bf16 v[44:47], v[96:99], v[240:243], v[44:47]\n"
    "v_add_f32 v119, v119, v79\n"
    "v_cvt_pk_bf16_f32 v31, v78, v79\n"
    "s_waitcnt lgkmcnt(0)\n"
    "v_mfma_f32_16x16x32_bf16 v[64:67], v[80:83], v[128:131], v[112:115]\n"
    "v_exp_f32 v32, v32\n"
    "v_mfma_f32_16x16x32_bf16 v[64:67], v[84:87], v[132:135], v[64:67]\n"
    "v_exp_f32 v33, v33\n"
    "v_mfma_f32_16x16x32_bf16 v[64:67], v[88:91], v[136:139], v[64:67]\n"
    "ds_read_b128 v[96:99], v120 offset:43008\n"
    "v_add_f32 v116, v116, v32\n"
    "v_mfma_f32_16x16x32_bf16 v[64:67], v[92:95], v[140:143], v[64:67]\n"
    "ds_read_b128 v[100:103], v121 offset:43008\n"
    "v_add_f32 v116, v116, v33\n"
    "v_mfma_f32_16x16x32_bf16 v[68:71], v[92:95], v[172:175], v[112:115]\n"
    "v_exp_f32 v34, v34\n"
    "v_mfma_f32_16x16x32_bf16 v[68:71], v[88:91], v[168:171], v[68:71]\n"
    "v_exp_f32 v35, v35\n"
    "v_mfma_f32_16x16x32_bf16 v[68:71], v[84:87], v[164:167], v[68:71]\n"
    "ds_read_b128 v[104:107], v120 offset:44032\n"
    "v_cvt_pk_bf16_f32 v32, v32, v33\n"
    "v_mfma_f32_16x16x32_bf16 v[68:71], v[80:83], v[160:163], v[68:71]\n"
    "ds_read_b128 v[108:111], v121 offset:44032\n"
    "v_add_f32 v116, v116, v34\n"
    "v_mfma_f32_16x16x32_bf16 v[72:75], v[80:83], v[192:195], v[112:115]\n"
    "v_exp_f32 v36, v36\n"
    "v_mfma_f32_16x16x32_bf16 v[72:75], v[84:87], v[196:199], v[72:75]\n"
    "v_exp_f32 v37, v37\n"
    "v_mfma_f32_16x16x32_bf16 v[72:75], v[88:91], v[200:203], v[72:75]\n"
    "v_add_f32 v116, v116, v35\n"
    "v_cvt_pk_bf16_f32 v33, v34, v35\n"
    "v_mfma_f32_16x16x32_bf16 v[72:75], v[92:95], v[204:207], v[72:75]\n"
    "v_exp_f32 v38, v38\n"
    "v_mfma_f32_16x16x32_bf16 v[76:79], v[92:95], v[236:239], v[112:115]\n"
    "v_exp_f32 v39, v39\n"
    "v_mfma_f32_16x16x32_bf16 v[76:79], v[88:91], v[232:235], v[76:79]\n"
    "v_add_f32 v117, v117, v36\n"
    "v_add_f32 v117, v117, v37\n"
    "v_mfma_f32_16x16x32_bf16 v[76:79], v[84:87], v[228:231], v[76:79]\n"
    "v_cvt_pk_bf16_f32 v36, v36, v37\n"
    "v_add_f32 v117, v117, v38\n"
    "v_mfma_f32_16x16x32_bf16 v[76:79], v[80:83], v[224:227], v[76:79]\n"
    "v_add_f32 v117, v117, v39\n"
    "v_cvt_pk_bf16_f32 v37, v38, v39\n"
    "s_waitcnt lgkmcnt(0)\n"
    "v_mfma_f32_16x16x32_bf16 v[64:67], v[96:99], v[144:147], v[64:67]\n"
    "v_exp_f32 v40, v40\n"
    "v_mfma_f32_16x16x32_bf16 v[64:67], v[100:103], v[148:151], v[64:67]\n"
    "v_exp_f32 v41, v41\n"
    "v_mfma_f32_16x16x32_bf16 v[64:67], v[104:107], v[152:155], v[64:67]\n"
    "ds_read_b128 v[80:83], v120 offset:49152\n"
    "v_add_f32 v118, v118, v40\n"
    "v_mfma_f32_16x16x32_bf16 v[64:67], v[108:111], v[156:159], v[64:67]\n"
    "ds_read_b128 v[84:87], v121 offset:49152\n"
    "v_add_f32 v118, v118, v41\n"
    "v_mfma_f32_16x16x32_bf16 v[68:71], v[108:111], v[188:191], v[68:71]\n"
    "v_exp_f32 v42, v42\n"
    "v_mfma_f32_16x16x32_bf16 v[68:71], v[104:107], v[184:187], v[68:71]\n"
    "v_exp_f32 v43, v43\n"
    "v_mfma_f32_16x16x32_bf16 v[68:71], v[100:103], v[180:183], v[68:71]\n"
    "ds_read_b128 v[88:91], v120 offset:50176\n"
    "v_cvt_pk_bf16_f32 v40, v40, v41\n"
    "v_mfma_f32_16x16x32_bf16 v[68:71], v[96:99], v[176:179], v[68:71]\n"
    "ds_read_b128 v[92:95], v121 offset:50176\n"
    "v_add_f32 v118, v118, v42\n"
    "v_mfma_f32_16x16x32_bf16 v[72:75], v[96:99], v[208:211], v[72:75]\n"
    "v_exp_f32 v44, v44\n"
    "v_mfma_f32_16x16x32_bf16 v[72:75], v[100:103], v[212:215], v[72:75]\n"
    "v_exp_f32 v45, v45\n"
    "v_mfma_f32_16x16x32_bf16 v[72:75], v[104:107], v[216:219], v[72:75]\n"
    "v_add_f32 v118, v118, v43\n"
    "v_cvt_pk_bf16_f32 v41, v42, v43\n"
    "v_mfma_f32_16x16x32_bf16 v[72:75], v[108:111], v[220:223], v[72:75]\n"
    "v_exp_f32 v46, v46\n"
    "v_mfma_f32_16x16x32_bf16 v[76:79], v[108:111], v[252:255], v[76:79]\n"
    "v_exp_f32 v47, v47\n"
    "v_mfma_f32_16x16x32_bf16 v[76:79], v[104:107], v[248:251], v[76:79]\n"
    "v_add_f32 v119, v119, v44\n"
    "v_add_f32 v119, v119, v45\n"
    "v_mfma_f32_16x16x32_bf16 v[76:79], v[100:103], v[244:247], v[76:79]\n"
    "v_cvt_pk_bf16_f32 v44, v44, v45\n"
    "v_add_f32 v119, v119, v46\n"
    "v_mfma_f32_16x16x32_bf16 v[76:79], v[96:99], v[240:243], v[76:79]\n"
    "v_add_f32 v119, v119, v47\n"
    "v_cvt_pk_bf16_f32 v45, v46, v47\n"
    "s_waitcnt lgkmcnt(0)\n"
    "v_mfma_f32_16x16x32_bf16 v[48:51], v[80:83], v[128:131], v[112:115]\n"
    "v_exp_f32 v64, v64\n"
    "v_mfma_f32_16x16x32_bf16 v[48:51], v[84:87], v[132:135], v[48:51]\n"
    "v_exp_f32 v65, v65\n"
    "v_mfma_f32_16x16x32_bf16 v[48:51], v[88:91], v[136:139], v[48:51]\n"
    "ds_read_b128 v[96:99], v120 offset:51200\n"
    "v_add_f32 v116, v116, v64\n"
    "v_mfma_f32_16x16x32_bf16 v[48:51], v[92:95], v[140:143], v[48:51]\n"
    "ds_read_b128 v[100:103], v121 offset:51200\n"
    "v_add_f32 v116, v116, v65\n"
    "v_mfma_f32_16x16x32_bf16 v[52:55], v[92:95], v[172:175], v[112:115]\n"
    "v_exp_f32 v66, v66\n"
    "v_mfma_f32_16x16x32_bf16 v[52:55], v[88:91], v[168:171], v[52:55]\n"
    "v_exp_f32 v67, v67\n"
    "v_mfma_f32_16x16x32_bf16 v[52:55], v[84:87], v[164:167], v[52:55]\n"
    "ds_read_b128 v[104:107], v120 offset:52224\n"
    "v_cvt_pk_bf16_f32 v34, v64, v65\n"
    "v_mfma_f32_16x16x32_bf16 v[52:55], v[80:83], v[160:163], v[52:55]\n"
    "ds_read_b128 v[108:111], v121 offset:52224\n"
    "v_add_f32 v116, v116, v66\n"
    "v_mfma_f32_16x16x32_bf16 v[56:59], v[80:83], v[192:195], v[112:115]\n"
    "v_exp_f32 v68, v68\n"
    "v_mfma_f32_16x16x32_bf16 v[56:59], v[84:87], v[196:199], v[56:59]\n"
    "v_exp_f32 v69, v69\n"
    "v_mfma_f32_16x16x32_bf16 v[56:59], v[88:91], v[200:203], v[56:59]\n"
    "v_add_f32 v116, v116, v67\n"
    "v_cvt_pk_bf16_f32 v35, v66, v67\n"
    "v_mfma_f32_16x16x32_bf16 v[56:59], v[92:95], v[204:207], v[56:59]\n"
    "v_exp_f32 v70, v70\n"
    "v_mfma_f32_16x16x32_bf16 v[60:63], v[92:95], v[236:239], v[112:115]\n"
    "v_exp_f32 v71, v71\n"
    "v_mfma_f32_16x16x32_bf16 v[60:63], v[88:91], v[232:235], v[60:63]\n"
    "v_add_f32 v117, v117, v68\n"
    "v_add_f32 v117, v117, v69\n"
    "v_mfma_f32_16x16x32_bf16 v[60:63], v[84:87], v[228:231], v[60:63]\n"
    "v_cvt_pk_bf16_f32 v38, v68, v69\n"
    "v_add_f32 v117, v117, v70\n"
    "v_mfma_f32_16x16x32_bf16 v[60:63], v[80:83], v[224:227], v[60:63]\n"
    "v_add_f32 v117, v117, v71\n"
    "v_cvt_pk_bf16_f32 v39, v70, v71\n"
    "s_waitcnt lgkmcnt(0)\n"
    "v_mfma_f32_16x16x32_bf16 v[48:51], v[96:99], v[144:147], v[48:51]\n"
    "v_exp_f32 v72, v72\n"
    "v_mfma_f32_16x16x32_bf16 v[48:51], v[100:103], v[148:151], v[48:51]\n"
    "v_exp_f32 v73, v73\n"
    "v_mfma_f32_16x16x32_bf16 v[48:51], v[104:107], v[152:155], v[48:51]\n"
    "ds_read_b128 v[80:83], v120 offset:57344\n"
    "v_add_f32 v118, v118, v72\n"
    "v_mfma_f32_16x16x32_bf16 v[48:51], v[108:111], v[156:159], v[48:51]\n"
    "ds_read_b128 v[84:87], v121 offset:57344\n"
    "v_add_f32 v118, v118, v73\n"
    "v_mfma_f32_16x16x32_bf16 v[52:55], v[108:111], v[188:191], v[52:55]\n"
    "v_exp_f32 v74, v74\n"
    "v_mfma_f32_16x16x32_bf16 v[52:55], v[104:107], v[184:187], v[52:55]\n"
    "v_exp_f32 v75, v75\n"
    "v_mfma_f32_16x16x32_bf16 v[52:55], v[100:103], v[180:183], v[52:55]\n"
    "ds_read_b128 v[88:91], v120 offset:58368\n"
    "v_cvt_pk_bf16_f32 v42, v72, v73\n"
    "v_mfma_f32_16x16x32_bf16 v[52:55], v[96:99], v[176:179], v[52:55]\n"
    "ds_read_b128 v[92:95], v121 offset:58368\n"
    "v_add_f32 v118, v118, v74\n"
    "v_mfma_f32_16x16x32_bf16 v[56:59], v[96:99], v[208:211], v[56:59]\n"
    "v_exp_f32 v76, v76\n"
    "v_mfma_f32_16x16x32_bf16 v[56:59], v[100:103], v[212:215], v[56:59]\n"
    "v_exp_f32 v77, v77\n"
    "v_mfma_f32_16x16x32_bf16 v[56:59], v[104:107], v[216:219], v[56:59]\n"
    "v_add_f32 v118, v118, v75\n"
    "v_cvt_pk_bf16_f32 v43, v74, v75\n"
    "v_mfma_f32_16x16x32_bf16 v[56:59], v[108:111], v[220:223], v[56:59]\n"
    "v_exp_f32 v78, v78\n"
    "v_mfma_f32_16x16x32_bf16 v[60:63], v[108:111], v[252:255], v[60:63]\n"
    "v_exp_f32 v79, v79\n"
    "v_mfma_f32_16x16x32_bf16 v[60:63], v[104:107], v[248:251], v[60:63]\n"
    "v_add_f32 v119, v119, v76\n"
    "v_add_f32 v119, v119, v77\n"
    "v_mfma_f32_16x16x32_bf16 v[60:63], v[100:103], v[244:247], v[60:63]\n"
    "v_cvt_pk_bf16_f32 v46, v76, v77\n"
    "v_add_f32 v119, v119, v78\n"
    "v_mfma_f32_16x16x32_bf16 v[60:63], v[96:99], v[240:243], v[60:63]\n"
    "v_add_f32 v119, v119, v79\n"
    "v_cvt_pk_bf16_f32 v47, v78, v79\n"
    "s_waitcnt lgkmcnt(0)\n"
    "v_mfma_f32_16x16x32_bf16 v[64:67], v[80:83], v[128:131], v[112:115]\n"
    "v_exp_f32 v48, v48\n"
    "v_mfma_f32_16x16x32_bf16 v[64:67], v[84:87], v[132:135], v[64:67]\n"
    "v_exp_f32 v49, v49\n"
    "v_mfma_f32_16x16x32_bf16 v[64:67], v[88:91], v[136:139], v[64:67]\n"
    "ds_read_b128 v[96:99], v120 offset:59392\n"
    "v_add_f32 v116, v116, v48\n"
    "v_mfma_f32_16x16x32_bf16 v[64:67], v[92:95], v[140:143], v[64:67]\n"
    "ds_read_b128 v[100:103], v121 offset:59392\n"
    "v_add_f32 v116, v116, v49\n"
    "v_mfma_f32_16x16x32_bf16 v[68:71], v[92:95], v[172:175], v[112:115]\n"
    "v_exp_f32 v50, v50\n"
    "v_mfma_f32_16x16x32_bf16 v[68:71], v[88:91], v[168:171], v[68:71]\n"
    "v_exp_f32 v51, v51\n"
    "v_mfma_f32_16x16x32_bf16 v[68:71], v[84:87], v[164:167], v[68:71]\n"
    "ds_read_b128 v[104:107], v120 offset:60416\n"
    "v_cvt_pk_bf16_f32 v48, v48, v49\n"
    "v_mfma_f32_16x16x32_bf16 v[68:71], v[80:83], v[160:163], v[68:71]\n"
    "ds_read_b128 v[108:111], v121 offset:60416\n"
    "v_add_f32 v116, v116, v50\n"
    "v_mfma_f32_16x16x32_bf16 v[72:75], v[80:83], v[192:195], v[112:115]\n"
    "v_exp_f32 v52, v52\n"
    "v_mfma_f32_16x16x32_bf16 v[72:75], v[84:87], v[196:199], v[72:75]\n"
    "v_exp_f32 v53, v53\n"
    "v_mfma_f32_16x16x32_bf16 v[72:75], v[88:91], v[200:203], v[72:75]\n"
    "v_add_f32 v116, v116, v51\n"
    "v_cvt_pk_bf16_f32 v49, v50, v51\n"
    "v_mfma_f32_16x16x32_bf16 v[72:75], v[92:95], v[204:207], v[72:75]\n"
    "v_exp_f32 v54, v54\n"
    "v_mfma_f32_16x16x32_bf16 v[76:79], v[92:95], v[236:239], v[112:115]\n"
    "v_exp_f32 v55, v55\n"
    "v_mfma_f32_16x16x32_bf16 v[76:79], v[88:91], v[232:235], v[76:79]\n"
    "v_add_f32 v117, v117, v52\n"
    "v_add_f32 v117, v117, v53\n"
    "v_mfma_f32_16x16x32_bf16 v[76:79], v[84:87], v[228:231], v[76:79]\n"
    "v_cvt_pk_bf16_f32 v52, v52, v53\n"
    "v_add_f32 v117, v117, v54\n"
    "v_mfma_f32_16x16x32_bf16 v[76:79], v[80:83], v[224:227], v[76:79]\n"
    "v_add_f32 v117, v117, v55\n"
    "v_cvt_pk_bf16_f32 v53, v54, v55\n"
    "s_waitcnt lgkmcnt(0)\n"
    "v_mfma_f32_16x16x32_bf16 v[64:67], v[96:99], v[144:147], v[64:67]\n"
    "v_exp_f32 v56, v56\n"
    "v_mfma_f32_16x16x32_bf16 v[64:67], v[100:103], v[148:151], v[64:67]\n"
    "v_exp_f32 v57, v57\n"
    "v_mfma_f32_16x16x32_bf16 v[64:67], v[104:107], v[152:155], v[64:67]\n"
    "ds_read_b64_tr_b16 v[80:81], v124 offset:32768\n"
    "v_add_f32 v118, v118, v56\n"
    "v_mfma_f32_16x16x32_bf16 v[64:67], v[108:111], v[156:159], v[64:67]\n"
    "ds_read_b64_tr_b16 v[82:83], v124 offset:40960\n"
    "v_add_f32 v118, v118, v57\n"
    "v_mfma_f32_16x16x32_bf16 v[68:71], v[108:111], v[188:191], v[68:71]\n"
    "v_exp_f32 v58, v58\n"
    "v_mfma_f32_16x16x32_bf16 v[68:71], v[104:107], v[184:187], v[68:71]\n"
    "v_exp_f32 v59, v59\n"
    "v_mfma_f32_16x16x32_bf16 v[68:71], v[100:103], v[180:183], v[68:71]\n"
    "ds_read_b64_tr_b16 v[84:85], v124 offset:49152\n"
    "v_cvt_pk_bf16_f32 v56, v56, v57\n"
    "v_mfma_f32_16x16x32_bf16 v[68:71], v[96:99], v[176:179], v[68:71]\n"
    "ds_read_b64_tr_b16 v[86:87], v124 offset:57344\n"
    "v_add_f32 v118, v118, v58\n"
    "v_mfma_f32_16x16x32_bf16 v[72:75], v[96:99], v[208:211], v[72:75]\n"
    "v_exp_f32 v60, v60\n"
    "v_mfma_f32_16x16x32_bf16 v[72:75], v[100:103], v[212:215], v[72:75]\n"
    "v_exp_f32 v61, v61\n"
    "v_mfma_f32_16x16x32_bf16 v[72:75], v[104:107], v[216:219], v[72:75]\n"
    "ds_read_b64_tr_b16 v[88:89], v125 offset:32768\n"
    "v_add_f32 v118, v118, v59\n"
    "v_cvt_pk_bf16_f32 v57, v58, v59\n"
    "v_mfma_f32_16x16x32_bf16 v[72:75], v[108:111], v[220:223], v[72:75]\n"
    "v_exp_f32 v62, v62\n"
    "v_mfma_f32_16x16x32_bf16 v[76:79], v[108:111], v[252:255], v[76:79]\n"
    "v_exp_f32 v63, v63\n"
    "v_mfma_f32_16x16x32_bf16 v[76:79], v[104:107], v[248:251], v[76:79]\n"
    "ds_read_b64_tr_b16 v[90:91], v125 offset:40960\n"
    "v_add_f32 v119, v119, v60\n"
    "v_add_f32 v119, v119, v61\n"
    "v_mfma_f32_16x16x32_bf16 v[76:79], v[100:103], v[244:247], v[76:79]\n"
    "ds_read_b64_tr_b16 v[92:93], v125 offset:49152\n"
    "v_cvt_pk_bf16_f32 v60, v60, v61\n"
    "v_add_f32 v119, v119, v62\n"
    "v_mfma_f32_16x16x32_bf16 v[76:79], v[96:99], v[240:243], v[76:79]\n"
    "ds_read_b64_tr_b16 v[94:95], v125 offset:57344\n"
    "v_add_f32 v119, v119, v63\n"
    "v_cvt_pk_bf16_f32 v61, v62, v63\n"
    "s_waitcnt lgkmcnt(4)\n"
    "v_mfma_f32_16x16x32_bf16 a[0:3], v[80:83], v[0:3], a[0:3]\n"
    "v_exp_f32 v64, v64\n"
    "v_mfma_f32_16x16x32_bf16 a[0:3], v[84:87], v[16:19], a[0:3]\n"
    "ds_read_b128 v[224:227], v127 offset:3072\n"
    "v_exp_f32 v65, v65\n"
    "v_mfma_f32_16x16x32_bf16 a[4:7], v[84:87], v[20:23], a[4:7]\n"
    "ds_read_b64_tr_b16 v[96:97], v126 offset:32768\n"
    "v_add_f32 v116, v116, v64\n"
    "v_mfma_f32_16x16x32_bf16 a[4:7], v[80:83], v[4:7], a[4:7]\n"
    "ds_read_b64_tr_b16 v[98:99], v126 offset:40960\n"
    "v_add_f32 v116, v116, v65\n"
    "v_mfma_f32_16x16x32_bf16 a[8:11], v[80:83], v[8:11], a[8:11]\n"
    "ds_read_b64_tr_b16 v[100:101], v126 offset:49152\n"
    "v_cvt_pk_bf16_f32 v50, v64, v65\n"
    "v_mfma_f32_16x16x32_bf16 a[8:11], v[84:87], v[24:27], a[8:11]\n"
    "ds_read_b64_tr_b16 v[102:103], v126 offset:57344\n"
    "v_add_u32 v216, 0xfffe8000, v124\n"
    "v_mfma_f32_16x16x32_bf16 a[12:15], v[84:87], v[28:31], a[12:15]\n"
    "ds_read_b128 v[128:131], v127 offset:0\n"
    "v_xor_b32 v217, 0x20, v216\n"
    "v_mfma_f32_16x16x32_bf16 a[12:15], v[80:83], v[12:15], a[12:15]\n"
    "ds_read_b128 v[132:135], v127 offset:4096\n"
    "s_waitcnt lgkmcnt(7)\n"
    "v_mfma_f32_16x16x32_bf16 a[16:19], v[88:91], v[0:3], a[16:19]\n"
    "v_exp_f32 v66, v66\n"
    "v_mfma_f32_16x16x32_bf16 a[16:19], v[92:95], v[16:19], a[16:19]\n"
    "ds_read_b128 v[228:231], v127 offset:7168\n"
    "v_exp_f32 v67, v67\n"
    "v_mfma_f32_16x16x32_bf16 a[20:23], v[92:95], v[20:23], a[20:23]\n"
    "ds_read_b64_tr_b16 v[104:105], v122 offset:32768\n"
    "v_add_f32 v116, v116, v66\n"
    "v_mfma_f32_16x16x32_bf16 a[20:23], v[88:91], v[4:7], a[20:23]\n"
    "ds_read_b64_tr_b16 v[106:107], v122 offset:40960\n"
    "v_add_f32 v116, v116, v67\n"
    "v_mfma_f32_16x16x32_bf16 a[24:27], v[88:91], v[8:11], a[24:27]\n"
    "ds_read_b64_tr_b16 v[108:109], v122 offset:49152\n"
    "v_cvt_pk_bf16_f32 v51, v66, v67\n"
    "v_mfma_f32_16x16x32_bf16 a[24:27], v[92:95], v[24:27], a[24:27]\n"
    "ds_read_b64_tr_b16 v[110:111], v122 offset:57344\n"
    "v_xor_b32 v218, 0x40, v216\n"
    "v_mfma_f32_16x16x32_bf16 a[28:31], v[92:95], v[28:31], a[28:31]\n"
    "ds_read_b128 v[160:163], v127 offset:1024\n"
    "v_xor_b32 v219, 0x60, v216\n"
    "v_mfma_f32_16x16x32_bf16 a[28:31], v[88:91], v[12:15], a[28:31]\n"
    "ds_read_b128 v[164:167], v127 offset:5120\n"
    "s_waitcnt lgkmcnt(9)\n"
    "v_mfma_f32_16x16x32_bf16 a[32:35], v[96:99], v[0:3], a[32:35]\n"
    "v_exp_f32 v68, v68\n"
    "v_mfma_f32_16x16x32_bf16 a[32:35], v[100:103], v[16:19], a[32:35]\n"
    "v_exp_f32 v69, v69\n"
    "v_mfma_f32_16x16x32_bf16 a[36:39], v[100:103], v[20:23], a[36:39]\n"
    "ds_read_b64_tr_b16 v[80:81], v124 offset:0\n"
    "v_add_f32 v117, v117, v68\n"
    "v_mfma_f32_16x16x32_bf16 a[36:39], v[96:99], v[4:7], a[36:39]\n"
    "ds_read_b64_tr_b16 v[82:83], v124 offset:8192\n"
    "v_add_f32 v117, v117, v69\n"
    "v_mfma_f32_16x16x32_bf16 a[40:43], v[96:99], v[8:11], a[40:43]\n"
    "ds_read_b64_tr_b16 v[84:85], v124 offset:16384\n"
    "v_cvt_pk_bf16_f32 v54, v68, v69\n"
    "v_mfma_f32_16x16x32_bf16 a[40:43], v[100:103], v[24:27], a[40:43]\n"
    "ds_read_b64_tr_b16 v[86:87], v124 offset:24576\n"
    "v_mbcnt_lo_u32_b32 v232, -1, 0\n"
    "v_mfma_f32_16x16x32_bf16 a[44:47], v[100:103], v[28:31], a[44:47]\n"
    "ds_read_b128 v[192:195], v127 offset:2048\n"
    "v_mbcnt_hi_u32_b32 v232, -1, v232\n"
    "v_mfma_f32_16x16x32_bf16 a[44:47], v[96:99], v[12:15], a[44:47]\n"
    "ds_read_b128 v[196:199], v127 offset:6144\n"
    "s_waitcnt lgkmcnt(8)\n"
    "v_mfma_f32_16x16x32_bf16 a[48:51], v[104:107], v[0:3], a[48:51]\n"
    "v_exp_f32 v70, v70\n"
    "v_mfma_f32_16x16x32_bf16 a[48:51], v[108:111], v[16:19], a[48:51]\n"
    "v_exp_f32 v71, v71\n"
    "v_mfma_f32_16x16x32_bf16 a[52:55], v[108:111], v[20:23], a[52:55]\n"
    "ds_read_b64_tr_b16 v[88:89], v125 offset:0\n"
    "v_add_f32 v117, v117, v70\n"
    "v_mfma_f32_16x16x32_bf16 a[52:55], v[104:107], v[4:7], a[52:55]\n"
    "ds_read_b64_tr_b16 v[90:91], v125 offset:8192\n"
    "v_add_f32 v117, v117, v71\n"
    "v_mfma_f32_16x16x32_bf16 a[56:59], v[104:107], v[8:11], a[56:59]\n"
    "ds_read_b64_tr_b16 v[92:93], v125 offset:16384\n"
    "v_cvt_pk_bf16_f32 v55, v70, v71\n"
    "v_mfma_f32_16x16x32_bf16 a[56:59], v[108:111], v[24:27], a[56:59]\n"
    "ds_read_b64_tr_b16 v[94:95], v125 offset:24576\n"
    "s_lshl_b32 s69, s20, 14\n"
    "v_mfma_f32_16x16x32_bf16 a[60:63], v[108:111], v[28:31], a[60:63]\n"
    "v_permlane32_swap_b32 v116, v117\n"
    "v_mfma_f32_16x16x32_bf16 a[60:63], v[104:107], v[12:15], a[60:63]\n"
    "v_add_f32 v116, v116, v117\n"
    "s_waitcnt lgkmcnt(4)\n"
    "v_mfma_f32_16x16x32_bf16 a[0:3], v[80:83], v[128:131], a[0:3]\n"
    "v_exp_f32 v72, v72\n"
    "v_mfma_f32_16x16x32_bf16 a[0:3], v[84:87], v[132:135], a[0:3]\n"
    "v_exp_f32 v73, v73\n"
    "v_mfma_f32_16x16x32_bf16 a[4:7], v[84:87], v[164:167], a[4:7]\n"
    "ds_read_b64_tr_b16 v[96:97], v126 offset:0\n"
    "v_add_f32 v118, v118, v72\n"
    "v_mfma_f32_16x16x32_bf16 a[4:7], v[80:83], v[160:163], a[4:7]\n"
    "ds_read_b64_tr_b16 v[98:99], v126 offset:8192\n"
    "v_add_f32 v118, v118, v73\n"
    "v_mfma_f32_16x16x32_bf16 a[8:11], v[80:83], v[192:195], a[8:11]\n"
    "ds_read_b64_tr_b16 v[100:101], v126 offset:16384\n"
    "v_cvt_pk_bf16_f32 v58, v72, v73\n"
    "v_mfma_f32_16x16x32_bf16 a[8:11], v[84:87], v[196:199], a[8:11]\n"
    "ds_read_b64_tr_b16 v[102:103], v126 offset:24576\n"
    "s_add_u32 s69, s69, s22\n"
    "v_mfma_f32_16x16x32_bf16 a[12:15], v[84:87], v[228:231], a[12:15]\n"
    "s_add_u32 s69, s69, 0x8000\n"
    "v_lshrrev_b32 v233, 5, v232\n"
    "v_mfma_f32_16x16x32_bf16 a[12:15], v[80:83], v[224:227], a[12:15]\n"
    "v_and_b32 v234, 7, v232\n"
    "s_waitcnt lgkmcnt(4)\n"
    "v_mfma_f32_16x16x32_bf16 a[16:19], v[88:91], v[128:131], a[16:19]\n"
    "v_exp_f32 v74, v74\n"
    "v_mfma_f32_16x16x32_bf16 a[16:19], v[92:95], v[132:135], a[16:19]\n"
    "v_exp_f32 v75, v75\n"
    "v_mfma_f32_16x16x32_bf16 a[20:23], v[92:95], v[164:167], a[20:23]\n"
    "ds_read_b64_tr_b16 v[104:105], v122 offset:0\n"
    "v_add_f32 v118, v118, v74\n"
    "v_mfma_f32_16x16x32_bf16 a[20:23], v[88:91], v[160:163], a[20:23]\n"
    "ds_read_b64_tr_b16 v[106:107], v122 offset:8192\n"
    "v_add_f32 v118, v118, v75\n"
    "v_mfma_f32_16x16x32_bf16 a[24:27], v[88:91], v[192:195], a[24:27]\n"
    "ds_read_b64_tr_b16 v[108:109], v122 offset:16384\n"
    "v_cvt_pk_bf16_f32 v59, v74, v75\n"
    "v_mfma_f32_16x16x32_bf16 a[24:27], v[92:95], v[196:199], a[24:27]\n"
    "ds_read_b64_tr_b16 v[110:111], v122 offset:24576\n"
    "v_xor_b32 v233, v233, v234\n"
    "v_mfma_f32_16x16x32_bf16 a[28:31], v[92:95], v[228:231], a[28:31]\n"
    "v_lshlrev_b32 v233, 4, v233\n"
    "v_and_b32 v234, 15, v232\n"
    "v_mfma_f32_16x16x32_bf16 a[28:31], v[88:91], v[224:227], a[28:31]\n"
    "v_lshl_add_u32 v233, v234, 7, v233\n"
    "s_waitcnt vmcnt(0) lgkmcnt(4)\n"
    "s_barrier\n"
    "v_mfma_f32_16x16x32_bf16 a[32:35], v[96:99], v[128:131], a[32:35]\n"
    "v_exp_f32 v76, v76\n"
    "v_mfma_f32_16x16x32_bf16 a[32:35], v[100:103], v[132:135], a[32:35]\n"
    "v_exp_f32 v77, v77\n"
    "v_mfma_f32_16x16x32_bf16 a[36:39], v[100:103], v[164:167], a[36:39]\n"
    "ds_read_b64_tr_b16 v[80:81], v216 offset:0\n"
    "v_add_f32 v119, v119, v76\n"
    "v_mfma_f32_16x16x32_bf16 a[36:39], v[96:99], v[160:163], a[36:39]\n"
    "ds_read_b64_tr_b16 v[82:83], v216 offset:8192\n"
    "v_add_f32 v119, v119, v77\n"
    "v_mfma_f32_16x16x32_bf16 a[40:43], v[96:99], v[192:195], a[40:43]\n"
    "ds_read_b64_tr_b16 v[84:85], v216 offset:16384\n"
    "v_cvt_pk_bf16_f32 v62, v76, v77\n"
    "v_mfma_f32_16x16x32_bf16 a[40:43], v[100:103], v[196:199], a[40:43]\n"
    "ds_read_b64_tr_b16 v[86:87], v216 offset:24576\n"
    "v_lshrrev_b32 v234, 1, v232\n"
    "v_mfma_f32_16x16x32_bf16 a[44:47], v[100:103], v[228:231], a[44:47]\n"
    "v_and_b32 v234, 8, v234\n"
    "v_add3_u32 v248, v233, v234, s69\n"
    "v_mfma_f32_16x16x32_bf16 a[44:47], v[96:99], v[224:227], a[44:47]\n"
    "v_xor_b32 v249, 0x20, v248\n"
    "s_waitcnt lgkmcnt(4)\n"
    "v_mfma_f32_16x16x32_bf16 a[48:51], v[104:107], v[128:131], a[48:51]\n"
    "v_exp_f32 v78, v78\n"
    "v_mfma_f32_16x16x32_bf16 a[48:51], v[108:111], v[132:135], a[48:51]\n"
    "v_exp_f32 v79, v79\n"
    "v_mfma_f32_16x16x32_bf16 a[52:55], v[108:111], v[164:167], a[52:55]\n"
    "ds_read_b64_tr_b16 v[88:89], v217 offset:0\n"
    "v_add_f32 v119, v119, v78\n"
    "v_mfma_f32_16x16x32_bf16 a[52:55], v[104:107], v[160:163], a[52:55]\n"
    "ds_read_b64_tr_b16 v[90:91], v217 offset:8192\n"
    "v_add_f32 v119, v119, v79\n"
    "v_mfma_f32_16x16x32_bf16 a[56:59], v[104:107], v[192:195], a[56:59]\n"
    "ds_read_b64_tr_b16 v[92:93], v217 offset:16384\n"
    "v_cvt_pk_bf16_f32 v63, v78, v79\n"
    "v_mfma_f32_16x16x32_bf16 a[56:59], v[108:111], v[196:199], a[56:59]\n"
    "ds_read_b64_tr_b16 v[94:95], v217 offset:24576\n"
    "v_xor_b32 v250, 0x40, v248\n"
    "v_mfma_f32_16x16x32_bf16 a[60:63], v[108:111], v[228:231], a[60:63]\n"
    "v_permlane32_swap_b32 v118, v119\n"
    "v_mfma_f32_16x16x32_bf16 a[60:63], v[104:107], v[224:227], a[60:63]\n"
    "v_add_f32 v118, v118, v119\n"
    "s_waitcnt lgkmcnt(4)\n"
    "v_mfma_f32_16x16x32_bf16 a[0:3], v[80:83], v[32:35], a[0:3]\n"
    "ds_read_b64_tr_b16 v[96:97], v218 offset:0\n"
    "v_xor_b32 v251, 0x60, v248\n"
    "v_mfma_f32_16x16x32_bf16 a[0:3], v[84:87], v[48:51], a[0:3]\n"
    "v_permlane16_swap_b32 v116, v118\n"
    "v_mfma_f32_16x16x32_bf16 a[4:7], v[84:87], v[52:55], a[4:7]\n"
    "ds_read_b64_tr_b16 v[98:99], v218 offset:8192\n"
    "v_add_f32 v116, v116, v118\n"
    "v_mfma_f32_16x16x32_bf16 a[4:7], v[80:83], v[36:39], a[4:7]\n"
    "v_rcp_f32 v112, v116\n"
    "v_mfma_f32_16x16x32_bf16 a[8:11], v[80:83], v[40:43], a[8:11]\n"
    "ds_read_b64_tr_b16 v[100:101], v218 offset:16384\n"
    "v_mov_b32 v114, v112\n"
    "v_mov_b32 v119, v113\n"
    "v_mfma_f32_16x16x32_bf16 a[8:11], v[84:87], v[56:59], a[8:11]\n"
    "v_permlane16_swap_b32 v112, v114\n"
    "v_accvgpr_read_b32 v136, a0\n"
    "v_mfma_f32_16x16x32_bf16 a[12:15], v[84:87], v[60:63], a[12:15]\n"
    "ds_read_b64_tr_b16 v[102:103], v218 offset:24576\n"
    "v_mov_b32 v113, v112\n"
    "v_mov_b32 v115, v114\n"
    "v_mfma_f32_16x16x32_bf16 a[12:15], v[80:83], v[44:47], a[12:15]\n"
    "v_permlane32_swap_b32 v112, v113\n"
    "v_permlane32_swap_b32 v114, v115\n"
    "s_waitcnt lgkmcnt(4)\n"
    "v_mfma_f32_16x16x32_bf16 a[16:19], v[88:91], v[32:35], a[16:19]\n"
    "ds_read_b64_tr_b16 v[104:105], v219 offset:0\n"
    "v_accvgpr_read_b32 v137, a1\n"
    "v_accvgpr_read_b32 v138, a2\n"
    "v_mfma_f32_16x16x32_bf16 a[16:19], v[92:95], v[48:51], a[16:19]\n"
    "v_accvgpr_read_b32 v139, a3\n"
    "v_mul_f32 v136, v112, v136\n"
    "v_mul_f32 v137, v112, v137\n"
    "v_mfma_f32_16x16x32_bf16 a[20:23], v[92:95], v[52:55], a[20:23]\n"
    "ds_read_b64_tr_b16 v[106:107], v219 offset:8192\n"
    "v_mul_f32 v138, v112, v138\n"
    "v_mul_f32 v139, v112, v139\n"
    "v_mfma_f32_16x16x32_bf16 a[20:23], v[88:91], v[36:39], a[20:23]\n"
    "v_cvt_pk_bf16_f32 v140, v136, v137\n"
    "v_cvt_pk_bf16_f32 v141, v138, v139\n"
    "v_accvgpr_read_b32 v142, a4\n"
    "v_mfma_f32_16x16x32_bf16 a[24:27], v[88:91], v[40:43], a[24:27]\n"
    "ds_read_b64_tr_b16 v[108:109], v219 offset:16384\n"
    "v_accvgpr_read_b32 v143, a5\n"
    "v_accvgpr_read_b32 v144, a6\n"
    "v_mfma_f32_16x16x32_bf16 a[24:27], v[92:95], v[56:59], a[24:27]\n"
    "ds_write_b64 v248, v[140:141] offset:0\n"
    "v_accvgpr_read_b32 v145, a7\n"
    "v_mul_f32 v142, v113, v142\n"
    "v_mfma_f32_16x16x32_bf16 a[28:31], v[92:95], v[60:63], a[28:31]\n"
    "ds_read_b64_tr_b16 v[110:111], v219 offset:24576\n"
    "v_mul_f32 v143, v113, v143\n"
    "v_mul_f32 v144, v113, v144\n"
    "v_mfma_f32_16x16x32_bf16 a[28:31], v[88:91], v[44:47], a[28:31]\n"
    "v_mul_f32 v145, v113, v145\n"
    "v_cvt_pk_bf16_f32 v146, v142, v143\n"
    "v_cvt_pk_bf16_f32 v147, v144, v145\n"
    "s_waitcnt lgkmcnt(5)\n"
    "v_mfma_f32_16x16x32_bf16 a[32:35], v[96:99], v[32:35], a[32:35]\n"
    "ds_read_b64_tr_b16 v[80:81], v124 offset:1024\n"
    "v_accvgpr_read_b32 v168, a8\n"
    "v_accvgpr_read_b32 v169, a9\n"
    "v_mfma_f32_16x16x32_bf16 a[32:35], v[100:103], v[48:51], a[32:35]\n"
    "ds_write_b64 v248, v[146:147] offset:2048\n"
    "v_accvgpr_read_b32 v170, a10\n"
    "v_accvgpr_read_b32 v171, a11\n"
    "v_mfma_f32_16x16x32_bf16 a[36:39], v[100:103], v[52:55], a[36:39]\n"
    "ds_read_b64_tr_b16 v[82:83], v124 offset:9216\n"
    "v_mul_f32 v168, v114, v168\n"
    "v_mul_f32 v169, v114, v169\n"
    "v_mfma_f32_16x16x32_bf16 a[36:39], v[96:99], v[36:39], a[36:39]\n"
    "v_mul_f32 v170, v114, v170\n"
    "v_mul_f32 v171, v114, v171\n"
    "v_cvt_pk_bf16_f32 v172, v168, v169\n"
    "v_mfma_f32_16x16x32_bf16 a[40:43], v[96:99], v[40:43], a[40:43]\n"
    "ds_read_b64_tr_b16 v[84:85], v124 offset:17408\n"
    "v_cvt_pk_bf16_f32 v173, v170, v171\n"
    "v_accvgpr_read_b32 v174, a12\n"
    "v_mfma_f32_16x16x32_bf16 a[40:43], v[100:103], v[56:59], a[40:43]\n"
    "ds_write_b64 v248, v[172:173] offset:4096\n"
    "v_accvgpr_read_b32 v175, a13\n"
    "v_accvgpr_read_b32 v176, a14\n"
    "v_mfma_f32_16x16x32_bf16 a[44:47], v[100:103], v[60:63], a[44:47]\n"
    "ds_read_b64_tr_b16 v[86:87], v124 offset:25600\n"
    "v_accvgpr_read_b32 v177, a15\n"
    "v_mul_f32 v174, v115, v174\n"
    "v_mfma_f32_16x16x32_bf16 a[44:47], v[96:99], v[44:47], a[44:47]\n"
    "v_mul_f32 v175, v115, v175\n"
    "v_mul_f32 v176, v115, v176\n"
    "v_mul_f32 v177, v115, v177\n"
    "s_waitcnt lgkmcnt(6)\n"
    "v_mfma_f32_16x16x32_bf16 a[48:51], v[104:107], v[32:35], a[48:51]\n"
    "ds_read_b64_tr_b16 v[88:89], v124 offset:33792\n"
    "v_cvt_pk_bf16_f32 v178, v174, v175\n"
    "v_cvt_pk_bf16_f32 v179, v176, v177\n"
    "v_mfma_f32_16x16x32_bf16 a[48:51], v[108:111], v[48:51], a[48:51]\n"
    "ds_write_b64 v248, v[178:179] offset:6144\n"
    "v_accvgpr_read_b32 v200, a16\n"
    "v_accvgpr_read_b32 v201, a17\n"
    "v_mfma_f32_16x16x32_bf16 a[52:55], v[108:111], v[52:55], a[52:55]\n"
    "ds_read_b64_tr_b16 v[90:91], v124 offset:41984\n"
    "v_accvgpr_read_b32 v202, a18\n"
    "v_accvgpr_read_b32 v203, a19\n"
    "v_mfma_f32_16x16x32_bf16 a[52:55], v[104:107], v[36:39], a[52:55]\n"
    "v_mul_f32 v200, v112, v200\n"
    "v_mul_f32 v201, v112, v201\n"
    "v_mul_f32 v202, v112, v202\n"
    "v_mfma_f32_16x16x32_bf16 a[56:59], v[104:107], v[40:43], a[56:59]\n"
    "ds_read_b64_tr_b16 v[92:93], v124 offset:50176\n"
    "v_mul_f32 v203, v112, v203\n"
    "v_cvt_pk_bf16_f32 v204, v200, v201\n"
    "v_mfma_f32_16x16x32_bf16 a[56:59], v[108:111], v[56:59], a[56:59]\n"
    "v_cvt_pk_bf16_f32 v205, v202, v203\n"
    "v_accvgpr_read_b32 v206, a20\n"
    "v_accvgpr_read_b32 v207, a21\n"
    "v_mfma_f32_16x16x32_bf16 a[60:63], v[108:111], v[60:63], a[60:63]\n"
    "ds_read_b64_tr_b16 v[94:95], v124 offset:58368\n"
    "v_accvgpr_read_b32 v208, a22\n"
    "v_accvgpr_read_b32 v209, a23\n"
    "v_mfma_f32_16x16x32_bf16 a[60:63], v[104:107], v[44:47], a[60:63]\n"
    "ds_write_b64 v249, v[204:205] offset:0\n"
    "v_mul_f32 v206, v113, v206\n"
    "v_mul_f32 v207, v113, v207\n"
    "s_waitcnt lgkmcnt(6)\n"
    "v_mfma_f32_16x16x32_bf16 a[64:67], v[80:83], v[128:131], a[64:67]\n"
    "ds_read_b64_tr_b16 v[96:97], v216 offset:1024\n"
    "v_mul_f32 v208, v113, v208\n"
    "v_mul_f32 v209, v113, v209\n"
    "v_mfma_f32_16x16x32_bf16 a[64:67], v[84:87], v[132:135], a[64:67]\n"
    "v_cvt_pk_bf16_f32 v210, v206, v207\n"
    "v_cvt_pk_bf16_f32 v211, v208, v209\n"
    "v_accvgpr_read_b32 v136, a24\n"
    "v_mfma_f32_16x16x32_bf16 a[68:71], v[84:87], v[164:167], a[68:71]\n"
    "ds_read_b64_tr_b16 v[98:99], v216 offset:9216\n"
    "v_accvgpr_read_b32 v137, a25\n"
    "v_accvgpr_read_b32 v138, a26\n"
    "v_mfma_f32_16x16x32_bf16 a[68:71], v[80:83], v[160:163], a[68:71]\n"
    "ds_write_b64 v249, v[210:211] offset:2048\n"
    "v_accvgpr_read_b32 v139, a27\n"
    "v_mul_f32 v136, v114, v136\n"
    "v_mfma_f32_16x16x32_bf16 a[72:75], v[80:83], v[192:195], a[72:75]\n"
    "ds_read_b64_tr_b16 v[100:101], v216 offset:17408\n"
    "v_mul_f32 v137, v114, v137\n"
    "v_mul_f32 v138, v114, v138\n"
    "v_mfma_f32_16x16x32_bf16 a[72:75], v[84:87], v[196:199], a[72:75]\n"
    "v_mul_f32 v139, v114, v139\n"
    "v_cvt_pk_bf16_f32 v140, v136, v137\n"
    "v_cvt_pk_bf16_f32 v141, v138, v139\n"
    "v_mfma_f32_16x16x32_bf16 a[76:79], v[84:87], v[228:231], a[76:79]\n"
    "ds_read_b64_tr_b16 v[102:103], v216 offset:25600\n"
    "v_accvgpr_read_b32 v142, a28\n"
    "v_accvgpr_read_b32 v143, a29\n"
    "v_mfma_f32_16x16x32_bf16 a[76:79], v[80:83], v[224:227], a[76:79]\n"
    "ds_write_b64 v249, v[140:141] offset:4096\n"
    "v_accvgpr_read_b32 v144, a30\n"
    "v_accvgpr_read_b32 v145, a31\n"
    "s_waitcnt lgkmcnt(7)\n"
    "v_mfma_f32_16x16x32_bf16 a[64:67], v[88:91], v[0:3], a[64:67]\n"
    "ds_read_b64_tr_b16 v[104:105], v125 offset:1024\n"
    "v_mul_f32 v142, v115, v142\n"
    "v_mul_f32 v143, v115, v143\n"
    "v_mfma_f32_16x16x32_bf16 a[64:67], v[92:95], v[16:19], a[64:67]\n"
    "v_mul_f32 v144, v115, v144\n"
    "v_mul_f32 v145, v115, v145\n"
    "v_cvt_pk_bf16_f32 v146, v142, v143\n"
    "v_mfma_f32_16x16x32_bf16 a[68:71], v[92:95], v[20:23], a[68:71]\n"
    "ds_read_b64_tr_b16 v[106:107], v125 offset:9216\n"
    "v_cvt_pk_bf16_f32 v147, v144, v145\n"
    "v_accvgpr_read_b32 v168, a32\n"
    "v_mfma_f32_16x16x32_bf16 a[68:71], v[88:91], v[4:7], a[68:71]\n"
    "ds_write_b64 v249, v[146:147] offset:6144\n"
    "v_accvgpr_read_b32 v169, a33\n"
    "v_accvgpr_read_b32 v170, a34\n"
    "v_mfma_f32_16x16x32_bf16 a[72:75], v[88:91], v[8:11], a[72:75]\n"
    "ds_read_b64_tr_b16 v[108:109], v125 offset:17408\n"
    "v_accvgpr_read_b32 v171, a35\n"
    "v_mul_f32 v168, v112, v168\n"
    "v_mfma_f32_16x16x32_bf16 a[72:75], v[92:95], v[24:27], a[72:75]\n"
    "v_mul_f32 v169, v112, v169\n"
    "v_mul_f32 v170, v112, v170\n"
    "v_mul_f32 v171, v112, v171\n"
    "v_mfma_f32_16x16x32_bf16 a[76:79], v[92:95], v[28:31], a[76:79]\n"
    "ds_read_b64_tr_b16 v[110:111], v125 offset:25600\n"
    "v_cvt_pk_bf16_f32 v172, v168, v169\n"
    "v_cvt_pk_bf16_f32 v173, v170, v171\n"
    "v_mfma_f32_16x16x32_bf16 a[76:79], v[88:91], v[12:15], a[76:79]\n"
    "ds_write_b64 v250, v[172:173] offset:0\n"
    "v_accvgpr_read_b32 v174, a36\n"
    "v_accvgpr_read_b32 v175, a37\n"
    "s_waitcnt lgkmcnt(7)\n"
    "v_mfma_f32_16x16x32_bf16 a[64:67], v[96:99], v[32:35], a[64:67]\n"
    "ds_read_b64_tr_b16 v[80:81], v125 offset:33792\n"
    "v_accvgpr_read_b32 v176, a38\n"
    "v_accvgpr_read_b32 v177, a39\n"
    "v_mfma_f32_16x16x32_bf16 a[64:67], v[100:103], v[48:51], a[64:67]\n"
    "v_mul_f32 v174, v113, v174\n"
    "v_mul_f32 v175, v113, v175\n"
    "v_mul_f32 v176, v113, v176\n"
    "v_mfma_f32_16x16x32_bf16 a[68:71], v[100:103], v[52:55], a[68:71]\n"
    "ds_read_b64_tr_b16 v[82:83], v125 offset:41984\n"
    "v_mul_f32 v177, v113, v177\n"
    "v_cvt_pk_bf16_f32 v178, v174, v175\n"
    "v_mfma_f32_16x16x32_bf16 a[68:71], v[96:99], v[36:39], a[68:71]\n"
    "v_cvt_pk_bf16_f32 v179, v176, v177\n"
    "v_accvgpr_read_b32 v200, a40\n"
    "v_accvgpr_read_b32 v201, a41\n"
    "v_mfma_f32_16x16x32_bf16 a[72:75], v[96:99], v[40:43], a[72:75]\n"
    "ds_read_b64_tr_b16 v[84:85], v125 offset:50176\n"
    "v_accvgpr_read_b32 v202, a42\n"
    "v_accvgpr_read_b32 v203, a43\n"
    "v_mfma_f32_16x16x32_bf16 a[72:75], v[100:103], v[56:59], a[72:75]\n"
    "ds_write_b64 v250, v[178:179] offset:2048\n"
    "v_mul_f32 v200, v114, v200\n"
    "v_mul_f32 v201, v114, v201\n"
    "v_mfma_f32_16x16x32_bf16 a[76:79], v[100:103], v[60:63], a[76:79]\n"
    "ds_read_b64_tr_b16 v[86:87], v125 offset:58368\n"
    "v_mul_f32 v202, v114, v202\n"
    "v_mul_f32 v203, v114, v203\n"
    "v_mfma_f32_16x16x32_bf16 a[76:79], v[96:99], v[44:47], a[76:79]\n"
    "v_cvt_pk_bf16_f32 v204, v200, v201\n"
    "v_cvt_pk_bf16_f32 v205, v202, v203\n"
    "v_accvgpr_read_b32 v206, a44\n"
    "s_waitcnt lgkmcnt(6)\n"
    "v_mfma_f32_16x16x32_bf16 a[80:83], v[104:107], v[128:131], a[80:83]\n"
    "ds_read_b64_tr_b16 v[88:89], v217 offset:1024\n"
    "v_accvgpr_read_b32 v207, a45\n"
    "v_accvgpr_read_b32 v208, a46\n"
    "v_mfma_f32_16x16x32_bf16 a[80:83], v[108:111], v[132:135], a[80:83]\n"
    "ds_write_b64 v250, v[204:205] offset:4096\n"
    "v_accvgpr_read_b32 v209, a47\n"
    "v_mul_f32 v206, v115, v206\n"
    "v_mfma_f32_16x16x32_bf16 a[84:87], v[108:111], v[164:167], a[84:87]\n"
    "ds_read_b64_tr_b16 v[90:91], v217 offset:9216\n"
    "v_mul_f32 v207, v115, v207\n"
    "v_mul_f32 v208, v115, v208\n"
    "v_mfma_f32_16x16x32_bf16 a[84:87], v[104:107], v[160:163], a[84:87]\n"
    "v_mul_f32 v209, v115, v209\n"
    "v_cvt_pk_bf16_f32 v210, v206, v207\n"
    "v_cvt_pk_bf16_f32 v211, v208, v209\n"
    "v_mfma_f32_16x16x32_bf16 a[88:91], v[104:107], v[192:195], a[88:91]\n"
    "ds_read_b64_tr_b16 v[92:93], v217 offset:17408\n"
    "v_lshrrev_b32 v233, 3, v232\n"
    "v_and_b32 v234, 7, v232\n"
    "v_mfma_f32_16x16x32_bf16 a[88:91], v[108:111], v[196:199], a[88:91]\n"
    "ds_write_b64 v250, v[210:211] offset:6144\n"
    "v_lshlrev_b32 v235, 4, v234\n"
    "v_lshl_add_u32 v235, v233, 9, v235\n"
    "v_mfma_f32_16x16x32_bf16 a[92:95], v[108:111], v[228:231], a[92:95]\n"
    "ds_read_b64_tr_b16 v[94:95], v217 offset:25600\n"
    "v_add_u32 v152, s21, v235\n"
    "v_add_u32 v153, 0x1000, v152\n"
    "v_mfma_f32_16x16x32_bf16 a[92:95], v[104:107], v[224:227], a[92:95]\n"
    "v_add_u32 v154, 0x2000, v152\n"
    "v_add_u32 v155, 0x3000, v152\n"
    "v_add_u32 v156, 0x4000, v152\n"
    "s_waitcnt lgkmcnt(6)\n"
    "v_mfma_f32_16x16x32_bf16 a[80:83], v[80:83], v[0:3], a[80:83]\n"
    "ds_read_b64_tr_b16 v[96:97], v126 offset:1024\n"
    "v_add_u32 v157, 0x5000, v152\n"
    "v_add_u32 v158, 0x6000, v152\n"
    "v_mfma_f32_16x16x32_bf16 a[80:83], v[84:87], v[16:19], a[80:83]\n"
    "v_add_u32 v159, 0x7000, v152\n"
    "v_xor_b32 v234, v234, v233\n"
    "v_lshlrev_b32 v234, 4, v234\n"
    "v_mfma_f32_16x16x32_bf16 a[84:87], v[84:87], v[20:23], a[84:87]\n"
    "ds_read_b64_tr_b16 v[98:99], v126 offset:9216\n"
    "v_lshl_add_u32 v234, v233, 7, v234\n"
    "v_add_u32 v252, s69, v234\n"
    "v_mfma_f32_16x16x32_bf16 a[84:87], v[80:83], v[4:7], a[84:87]\n"
    "v_accvgpr_read_b32 v136, a48\n"
    "v_accvgpr_read_b32 v137, a49\n"
    "v_accvgpr_read_b32 v138, a50\n"
    "v_mfma_f32_16x16x32_bf16 a[88:91], v[80:83], v[8:11], a[88:91]\n"
    "ds_read_b64_tr_b16 v[100:101], v126 offset:17408\n"
    "v_accvgpr_read_b32 v139, a51\n"
    "v_mul_f32 v136, v112, v136\n"
    "v_mfma_f32_16x16x32_bf16 a[88:91], v[84:87], v[24:27], a[88:91]\n"
    "v_mul_f32 v137, v112, v137\n"
    "v_mul_f32 v138, v112, v138\n"
    "v_mul_f32 v139, v112, v139\n"
    "v_mfma_f32_16x16x32_bf16 a[92:95], v[84:87], v[28:31], a[92:95]\n"
    "ds_read_b64_tr_b16 v[102:103], v126 offset:25600\n"
    "v_cvt_pk_bf16_f32 v140, v136, v137\n"
    "v_cvt_pk_bf16_f32 v141, v138, v139\n"
    "v_mfma_f32_16x16x32_bf16 a[92:95], v[80:83], v[12:15], a[92:95]\n"
    "ds_write_b64 v251, v[140:141] offset:0\n"
    "v_accvgpr_read_b32 v142, a52\n"
    "v_accvgpr_read_b32 v143, a53\n"
    "s_waitcnt lgkmcnt(5)\n"
    "v_mfma_f32_16x16x32_bf16 a[80:83], v[88:91], v[32:35], a[80:83]\n"
    "ds_read_b64_tr_b16 v[104:105], v126 offset:33792\n"
    "v_accvgpr_read_b32 v144, a54\n"
    "v_accvgpr_read_b32 v145, a55\n"
    "v_mfma_f32_16x16x32_bf16 a[80:83], v[92:95], v[48:51], a[80:83]\n"
    "ds_read_b128 v[184:187], v252 offset:0\n"
    "v_mul_f32 v142, v113, v142\n"
    "v_mul_f32 v143, v113, v143\n"
    "v_mfma_f32_16x16x32_bf16 a[84:87], v[92:95], v[52:55], a[84:87]\n"
    "ds_read_b64_tr_b16 v[106:107], v126 offset:41984\n"
    "v_mul_f32 v144, v113, v144\n"
    "v_mul_f32 v145, v113, v145\n"
    "v_mfma_f32_16x16x32_bf16 a[84:87], v[88:91], v[36:39], a[84:87]\n"
    "ds_read_b128 v[188:191], v252 offset:1024\n"
    "v_cvt_pk_bf16_f32 v146, v142, v143\n"
    "v_cvt_pk_bf16_f32 v147, v144, v145\n"
    "v_mfma_f32_16x16x32_bf16 a[88:91], v[88:91], v[40:43], a[88:91]\n"
    "ds_read_b64_tr_b16 v[108:109], v126 offset:50176\n"
    "v_accvgpr_read_b32 v168, a56\n"
    "v_accvgpr_read_b32 v169, a57\n"
    "v_mfma_f32_16x16x32_bf16 a[88:91], v[92:95], v[56:59], a[88:91]\n"
    "ds_write_b64 v251, v[146:147] offset:2048\n"
    "v_accvgpr_read_b32 v170, a58\n"
    "v_accvgpr_read_b32 v171, a59\n"
    "v_mfma_f32_16x16x32_bf16 a[92:95], v[92:95], v[60:63], a[92:95]\n"
    "ds_read_b64_tr_b16 v[110:111], v126 offset:58368\n"
    "v_mul_f32 v168, v114, v168\n"
    "v_mul_f32 v169, v114, v169\n"
    "v_mfma_f32_16x16x32_bf16 a[92:95], v[88:91], v[44:47], a[92:95]\n"
    "ds_read_b128 v[64:67], v252 offset:2048\n"
    "v_mul_f32 v170, v114, v170\n"
    "v_mul_f32 v171, v114, v171\n"
    "s_waitcnt lgkmcnt(9)\n"
    "v_mfma_f32_16x16x32_bf16 a[96:99], v[96:99], v[128:131], a[96:99]\n"
    "ds_read_b64_tr_b16 v[80:81], v218 offset:1024\n"
    "s_waitcnt lgkmcnt(5)\n"
    "buffer_store_dwordx4 v[184:187], v152, s[52:55], 0 offen offset:0 nt\n"
    "v_mfma_f32_16x16x32_bf16 a[96:99], v[100:103], v[132:135], a[96:99]\n"
    "ds_read_b128 v[68:71], v252 offset:3072\n"
    "v_cvt_pk_bf16_f32 v172, v168, v169\n"
    "v_cvt_pk_bf16_f32 v173, v170, v171\n"
    "v_mfma_f32_16x16x32_bf16 a[100:103], v[100:103], v[164:167], a[100:103]\n"
    "ds_read_b64_tr_b16 v[82:83], v218 offset:9216\n"
    "v_accvgpr_read_b32 v174, a60\n"
    "v_accvgpr_read_b32 v175, a61\n"
    "v_mfma_f32_16x16x32_bf16 a[100:103], v[96:99], v[160:163], a[100:103]\n"
    "ds_write_b64 v251, v[172:173] offset:4096\n"
    "v_accvgpr_read_b32 v176, a62\n"
    "v_accvgpr_read_b32 v177, a63\n"
    "v_mfma_f32_16x16x32_bf16 a[104:107], v[96:99], v[192:195], a[104:107]\n"
    "ds_read_b64_tr_b16 v[84:85], v218 offset:17408\n"
    "v_mul_f32 v174, v115, v174\n"
    "v_mul_f32 v175, v115, v175\n"
    "v_mfma_f32_16x16x32_bf16 a[104:107], v[100:103], v[196:199], a[104:107]\n"
    "ds_read_b128 v[72:75], v252 offset:4096\n"
    "v_mul_f32 v176, v115, v176\n"
    "v_mul_f32 v177, v115, v177\n"
    "v_mfma_f32_16x16x32_bf16 a[108:111], v[100:103], v[228:231], a[108:111]\n"
    "ds_read_b64_tr_b16 v[86:87], v218 offset:25600\n"
    "v_cvt_pk_bf16_f32 v178, v174, v175\n"
    "v_cvt_pk_bf16_f32 v179, v176, v177\n"
    "v_mfma_f32_16x16x32_bf16 a[108:111], v[96:99], v[224:227], a[108:111]\n"
    "ds_read_b128 v[76:79], v252 offset:5120\n"
    "v_accvgpr_read_b32 v200, a64\n"
    "v_accvgpr_read_b32 v201, a65\n"
    "s_waitcnt lgkmcnt(9)\n"
    "v_mfma_f32_16x16x32_bf16 a[96:99], v[104:107], v[0:3], a[96:99]\n"
    "ds_read_b64_tr_b16 v[88:89], v122 offset:1024\n"
    "v_accvgpr_read_b32 v202, a66\n"
    "v_accvgpr_read_b32 v203, a67\n"
    "v_mfma_f32_16x16x32_bf16 a[96:99], v[108:111], v[16:19], a[96:99]\n"
    "ds_write_b64 v251, v[178:179] offset:6144\n"
    "v_mul_f32 v200, v112, v200\n"
    "v_mul_f32 v201, v112, v201\n"
    "v_mfma_f32_16x16x32_bf16 a[100:103], v[108:111], v[20:23], a[100:103]\n"
    "ds_read_b64_tr_b16 v[90:91], v122 offset:9216\n"
    "buffer_store_dwordx4 v[188:191], v153, s[52:55], 0 offen offset:0 nt\n"
    "v_mul_f32 v202, v112, v202\n"
    "v_mfma_f32_16x16x32_bf16 a[100:103], v[104:107], v[4:7], a[100:103]\n"
    "ds_read_b128 v[240:243], v252 offset:6144\n"
    "v_mul_f32 v203, v112, v203\n"
    "v_cvt_pk_bf16_f32 v204, v200, v201\n"
    "v_mfma_f32_16x16x32_bf16 a[104:107], v[104:107], v[8:11], a[104:107]\n"
    "ds_read_b64_tr_b16 v[92:93], v122 offset:17408\n"
    "v_cvt_pk_bf16_f32 v205, v202, v203\n"
    "v_accvgpr_read_b32 v206, a68\n"
    "v_mfma_f32_16x16x32_bf16 a[104:107], v[108:111], v[24:27], a[104:107]\n"
    "ds_read_b128 v[244:247], v252 offset:7168\n"
    "v_accvgpr_read_b32 v207, a69\n"
    "v_accvgpr_read_b32 v208, a70\n"
    "v_mfma_f32_16x16x32_bf16 a[108:111], v[108:111], v[28:31], a[108:111]\n"
    "ds_read_b64_tr_b16 v[94:95], v122 offset:25600\n"
    "v_accvgpr_read_b32 v209, a71\n"
    "v_mul_f32 v206, v113, v206\n"
    "v_mfma_f32_16x16x32_bf16 a[108:111], v[104:107], v[12:15], a[108:111]\n"
    "ds_write_b64 v248, v[204:205] offset:8192\n"
    "v_mul_f32 v207, v113, v207\n"
    "v_mul_f32 v208, v113, v208\n"
    "s_waitcnt lgkmcnt(9)\n"
    "v_mfma_f32_16x16x32_bf16 a[96:99], v[80:83], v[32:35], a[96:99]\n"
    "ds_read_b64_tr_b16 v[96:97], v122 offset:33792\n"
    "v_mul_f32 v209, v113, v209\n"
    "v_cvt_pk_bf16_f32 v210, v206, v207\n"
    "v_mfma_f32_16x16x32_bf16 a[96:99], v[84:87], v[48:51], a[96:99]\n"
    "v_cvt_pk_bf16_f32 v211, v208, v209\n"
    "v_accvgpr_read_b32 v136, a72\n"
    "v_accvgpr_read_b32 v137, a73\n"
    "v_mfma_f32_16x16x32_bf16 a[100:103], v[84:87], v[52:55], a[100:103]\n"
    "ds_read_b64_tr_b16 v[98:99], v122 offset:41984\n"
    "v_accvgpr_read_b32 v138, a74\n"
    "v_accvgpr_read_b32 v139, a75\n"
    "v_mfma_f32_16x16x32_bf16 a[100:103], v[80:83], v[36:39], a[100:103]\n"
    "ds_write_b64 v248, v[210:211] offset:10240\n"
    "v_mul_f32 v136, v114, v136\n"
    "v_mul_f32 v137, v114, v137\n"
    "v_mfma_f32_16x16x32_bf16 a[104:107], v[80:83], v[40:43], a[104:107]\n"
    "ds_read_b64_tr_b16 v[100:101], v122 offset:50176\n"
    "s_waitcnt lgkmcnt(15)\n"
    "buffer_store_dwordx4 v[64:67], v154, s[52:55], 0 offen offset:0 nt\n"
    "v_mfma_f32_16x16x32_bf16 a[104:107], v[84:87], v[56:59], a[104:107]\n"
    "v_mul_f32 v138, v114, v138\n"
    "v_mul_f32 v139, v114, v139\n"
    "v_cvt_pk_bf16_f32 v140, v136, v137\n"
    "v_mfma_f32_16x16x32_bf16 a[108:111], v[84:87], v[60:63], a[108:111]\n"
    "ds_read_b64_tr_b16 v[102:103], v122 offset:58368\n"
    "v_cvt_pk_bf16_f32 v141, v138, v139\n"
    "v_accvgpr_read_b32 v142, a76\n"
    "v_mfma_f32_16x16x32_bf16 a[108:111], v[80:83], v[44:47], a[108:111]\n"
    "ds_write_b64 v248, v[140:141] offset:12288\n"
    "v_accvgpr_read_b32 v143, a77\n"
    "v_accvgpr_read_b32 v144, a78\n"
    "s_waitcnt lgkmcnt(7)\n"
    "v_mfma_f32_16x16x32_bf16 a[112:115], v[88:91], v[128:131], a[112:115]\n"
    "ds_read_b64_tr_b16 v[104:105], v219 offset:1024\n"
    "v_accvgpr_read_b32 v145, a79\n"
    "v_mul_f32 v142, v115, v142\n"
    "v_mfma_f32_16x16x32_bf16 a[112:115], v[92:95], v[132:135], a[112:115]\n"
    "v_mul_f32 v143, v115, v143\n"
    "v_mul_f32 v144, v115, v144\n"
    "v_mul_f32 v145, v115, v145\n"
    "v_mfma_f32_16x16x32_bf16 a[116:119], v[92:95], v[164:167], a[116:119]\n"
    "ds_read_b64_tr_b16 v[106:107], v219 offset:9216\n"
    "v_cvt_pk_bf16_f32 v146, v142, v143\n"
    "v_cvt_pk_bf16_f32 v147, v144, v145\n"
    "v_mfma_f32_16x16x32_bf16 a[116:119], v[88:91], v[160:163], a[116:119]\n"
    "ds_write_b64 v248, v[146:147] offset:14336\n"
    "v_accvgpr_read_b32 v168, a80\n"
    "v_accvgpr_read_b32 v169, a81\n"
    "v_mfma_f32_16x16x32_bf16 a[120:123], v[88:91], v[192:195], a[120:123]\n"
    "ds_read_b64_tr_b16 v[108:109], v219 offset:17408\n"
    "v_accvgpr_read_b32 v170, a82\n"
    "v_accvgpr_read_b32 v171, a83\n"
    "v_mfma_f32_16x16x32_bf16 a[120:123], v[92:95], v[196:199], a[120:123]\n"
    "v_mul_f32 v168, v112, v168\n"
    "v_mul_f32 v169, v112, v169\n"
    "v_mul_f32 v170, v112, v170\n"
    "v_mfma_f32_16x16x32_bf16 a[124:127], v[92:95], v[228:231], a[124:127]\n"
    "ds_read_b64_tr_b16 v[110:111], v219 offset:25600\n"
    "buffer_store_dwordx4 v[68:71], v155, s[52:55], 0 offen offset:0 nt\n"
    "v_mul_f32 v171, v112, v171\n"
    "v_mfma_f32_16x16x32_bf16 a[124:127], v[88:91], v[224:227], a[124:127]\n"
    "v_cvt_pk_bf16_f32 v172, v168, v169\n"
    "v_cvt_pk_bf16_f32 v173, v170, v171\n"
    "v_accvgpr_read_b32 v174, a84\n"
    "s_waitcnt lgkmcnt(6)\n"
    "v_mfma_f32_16x16x32_bf16 a[112:115], v[96:99], v[0:3], a[112:115]\n"
    "ds_read_b64_tr_b16 v[80:81], v124 offset:2048\n"
    "v_accvgpr_read_b32 v175, a85\n"
    "v_accvgpr_read_b32 v176, a86\n"
    "v_mfma_f32_16x16x32_bf16 a[112:115], v[100:103], v[16:19], a[112:115]\n"
    "ds_write_b64 v249, v[172:173] offset:8192\n"
    "v_accvgpr_read_b32 v177, a87\n"
    "v_mul_f32 v174, v113, v174\n"
    "v_mfma_f32_16x16x32_bf16 a[116:119], v[100:103], v[20:23], a[116:119]\n"
    "ds_read_b64_tr_b16 v[82:83], v124 offset:10240\n"
    "v_mul_f32 v175, v113, v175\n"
    "v_mul_f32 v176, v113, v176\n"
    "v_mfma_f32_16x16x32_bf16 a[116:119], v[96:99], v[4:7], a[116:119]\n"
    "v_mul_f32 v177, v113, v177\n"
    "v_cvt_pk_bf16_f32 v178, v174, v175\n"
    "v_cvt_pk_bf16_f32 v179, v176, v177\n"
    "v_mfma_f32_16x16x32_bf16 a[120:123], v[96:99], v[8:11], a[120:123]\n"
    "ds_read_b64_tr_b16 v[84:85], v124 offset:18432\n"
    "v_accvgpr_read_b32 v200, a88\n"
    "v_accvgpr_read_b32 v201, a89\n"
    "v_mfma_f32_16x16x32_bf16 a[120:123], v[100:103], v[24:27], a[120:123]\n"
    "ds_write_b64 v249, v[178:179] offset:10240\n"
    "v_accvgpr_read_b32 v202, a90\n"
    "v_accvgpr_read_b32 v203, a91\n"
    "v_mfma_f32_16x16x32_bf16 a[124:127], v[100:103], v[28:31], a[124:127]\n"
    "ds_read_b64_tr_b16 v[86:87], v124 offset:26624\n"
    "v_mul_f32 v200, v114, v200\n"
    "v_mul_f32 v201, v114, v201\n"
    "v_mfma_f32_16x16x32_bf16 a[124:127], v[96:99], v[12:15], a[124:127]\n"
    "v_mul_f32 v202, v114, v202\n"
    "v_mul_f32 v203, v114, v203\n"
    "v_cvt_pk_bf16_f32 v204, v200, v201\n"
    "s_waitcnt lgkmcnt(6)\n"
    "v_mfma_f32_16x16x32_bf16 a[112:115], v[104:107], v[32:35], a[112:115]\n"
    "ds_read_b64_tr_b16 v[88:89], v124 offset:34816\n"
    "s_waitcnt lgkmcnt(15)\n"
    "buffer_store_dwordx4 v[72:75], v156, s[52:55], 0 offen offset:0 nt\n"
    "v_mfma_f32_16x16x32_bf16 a[112:115], v[108:111], v[48:51], a[112:115]\n"
    "v_cvt_pk_bf16_f32 v205, v202, v203\n"
    "v_accvgpr_read_b32 v206, a92\n"
    "v_accvgpr_read_b32 v207, a93\n"
    "v_mfma_f32_16x16x32_bf16 a[116:119], v[108:111], v[52:55], a[116:119]\n"
    "ds_read_b64_tr_b16 v[90:91], v124 offset:43008\n"
    "v_accvgpr_read_b32 v208, a94\n"
    "v_accvgpr_read_b32 v209, a95\n"
    "v_mfma_f32_16x16x32_bf16 a[116:119], v[104:107], v[36:39], a[116:119]\n"
    "ds_write_b64 v249, v[204:205] offset:12288\n"
    "v_mul_f32 v206, v115, v206\n"
    "v_mul_f32 v207, v115, v207\n"
    "v_mfma_f32_16x16x32_bf16 a[120:123], v[104:107], v[40:43], a[120:123]\n"
    "ds_read_b64_tr_b16 v[92:93], v124 offset:51200\n"
    "v_mul_f32 v208, v115, v208\n"
    "v_mul_f32 v209, v115, v209\n"
    "v_mfma_f32_16x16x32_bf16 a[120:123], v[108:111], v[56:59], a[120:123]\n"
    "v_cvt_pk_bf16_f32 v210, v206, v207\n"
    "v_cvt_pk_bf16_f32 v211, v208, v209\n"
    "v_accvgpr_read_b32 v136, a96\n"
    "v_mfma_f32_16x16x32_bf16 a[124:127], v[108:111], v[60:63], a[124:127]\n"
    "ds_read_b64_tr_b16 v[94:95], v124 offset:59392\n"
    "v_accvgpr_read_b32 v137, a97\n"
    "v_accvgpr_read_b32 v138, a98\n"
    "v_mfma_f32_16x16x32_bf16 a[124:127], v[104:107], v[44:47], a[124:127]\n"
    "ds_write_b64 v249, v[210:211] offset:14336\n"
    "v_accvgpr_read_b32 v139, a99\n"
    "v_mul_f32 v136, v112, v136\n"
    "s_waitcnt lgkmcnt(6)\n"
    "v_mfma_f32_16x16x32_bf16 a[128:131], v[80:83], v[128:131], a[128:131]\n"
    "ds_read_b64_tr_b16 v[96:97], v216 offset:2048\n"
    "v_mul_f32 v137, v112, v137\n"
    "v_mul_f32 v138, v112, v138\n"
    "v_mfma_f32_16x16x32_bf16 a[128:131], v[84:87], v[132:135], a[128:131]\n"
    "v_mul_f32 v139, v112, v139\n"
    "v_cvt_pk_bf16_f32 v140, v136, v137\n"
    "v_cvt_pk_bf16_f32 v141, v138, v139\n"
    "v_mfma_f32_16x16x32_bf16 a[132:135], v[84:87], v[164:167], a[132:135]\n"
    "ds_read_b64_tr_b16 v[98:99], v216 offset:10240\n"
    "buffer_store_dwordx4 v[76:79], v157, s[52:55], 0 offen offset:0 nt\n"
    "v_accvgpr_read_b32 v142, a100\n"
    "v_mfma_f32_16x16x32_bf16 a[132:135], v[80:83], v[160:163], a[132:135]\n"
    "ds_write_b64 v250, v[140:141] offset:8192\n"
    "v_accvgpr_read_b32 v143, a101\n"
    "v_accvgpr_read_b32 v144, a102\n"
    "v_mfma_f32_16x16x32_bf16 a[136:139], v[80:83], v[192:195], a[136:139]\n"
    "ds_read_b64_tr_b16 v[100:101], v216 offset:18432\n"
    "v_accvgpr_read_b32 v145, a103\n"
    "v_mul_f32 v142, v113, v142\n"
    "v_mfma_f32_16x16x32_bf16 a[136:139], v[84:87], v[196:199], a[136:139]\n"
    "v_mul_f32 v143, v113, v143\n"
    "v_mul_f32 v144, v113, v144\n"
    "v_mul_f32 v145, v113, v145\n"
    "v_mfma_f32_16x16x32_bf16 a[140:143], v[84:87], v[228:231], a[140:143]\n"
    "ds_read_b64_tr_b16 v[102:103], v216 offset:26624\n"
    "v_cvt_pk_bf16_f32 v146, v142, v143\n"
    "v_cvt_pk_bf16_f32 v147, v144, v145\n"
    "v_mfma_f32_16x16x32_bf16 a[140:143], v[80:83], v[224:227], a[140:143]\n"
    "ds_write_b64 v250, v[146:147] offset:10240\n"
    "v_accvgpr_read_b32 v168, a104\n"
    "v_accvgpr_read_b32 v169, a105\n"
    "s_waitcnt lgkmcnt(7)\n"
    "v_mfma_f32_16x16x32_bf16 a[128:131], v[88:91], v[0:3], a[128:131]\n"
    "ds_read_b64_tr_b16 v[104:105], v125 offset:2048\n"
    "v_accvgpr_read_b32 v170, a106\n"
    "v_accvgpr_read_b32 v171, a107\n"
    "v_mfma_f32_16x16x32_bf16 a[128:131], v[92:95], v[16:19], a[128:131]\n"
    "v_mul_f32 v168, v114, v168\n"
    "v_mul_f32 v169, v114, v169\n"
    "v_mul_f32 v170, v114, v170\n"
    "v_mfma_f32_16x16x32_bf16 a[132:135], v[92:95], v[20:23], a[132:135]\n"
    "ds_read_b64_tr_b16 v[106:107], v125 offset:10240\n"
    "v_mul_f32 v171, v114, v171\n"
    "v_cvt_pk_bf16_f32 v172, v168, v169\n"
    "v_mfma_f32_16x16x32_bf16 a[132:135], v[88:91], v[4:7], a[132:135]\n"
    "v_cvt_pk_bf16_f32 v173, v170, v171\n"
    "v_accvgpr_read_b32 v174, a108\n"
    "v_accvgpr_read_b32 v175, a109\n"
    "v_mfma_f32_16x16x32_bf16 a[136:139], v[88:91], v[8:11], a[136:139]\n"
    "ds_read_b64_tr_b16 v[108:109], v125 offset:18432\n"
    "s_waitcnt lgkmcnt(15)\n"
    "buffer_store_dwordx4 v[240:243], v158, s[52:55], 0 offen offset:0 nt\n"
    "v_mfma_f32_16x16x32_bf16 a[136:139], v[92:95], v[24:27], a[136:139]\n"
    "ds_write_b64 v250, v[172:173] offset:12288\n"
    "v_accvgpr_read_b32 v176, a110\n"
    "v_accvgpr_read_b32 v177, a111\n"
    "v_mfma_f32_16x16x32_bf16 a[140:143], v[92:95], v[28:31], a[140:143]\n"
    "ds_read_b64_tr_b16 v[110:111], v125 offset:26624\n"
    "v_mul_f32 v174, v115, v174\n"
    "v_mul_f32 v175, v115, v175\n"
    "v_mfma_f32_16x16x32_bf16 a[140:143], v[88:91], v[12:15], a[140:143]\n"
    "v_mul_f32 v176, v115, v176\n"
    "v_mul_f32 v177, v115, v177\n"
    "v_cvt_pk_bf16_f32 v178, v174, v175\n"
    "s_waitcnt lgkmcnt(6)\n"
    "v_mfma_f32_16x16x32_bf16 a[128:131], v[96:99], v[32:35], a[128:131]\n"
    "ds_read_b64_tr_b16 v[80:81], v125 offset:34816\n"
    "v_cvt_pk_bf16_f32 v179, v176, v177\n"
    "v_accvgpr_read_b32 v200, a112\n"
    "v_mfma_f32_16x16x32_bf16 a[128:131], v[100:103], v[48:51], a[128:131]\n"
    "ds_write_b64 v250, v[178:179] offset:14336\n"
    "v_accvgpr_read_b32 v201, a113\n"
    "v_accvgpr_read_b32 v202, a114\n"
    "v_mfma_f32_16x16x32_bf16 a[132:135], v[100:103], v[52:55], a[132:135]\n"
    "ds_read_b64_tr_b16 v[82:83], v125 offset:43008\n"
    "v_accvgpr_read_b32 v203, a115\n"
    "v_mul_f32 v200, v112, v200\n"
    "v_mfma_f32_16x16x32_bf16 a[132:135], v[96:99], v[36:39], a[132:135]\n"
    "v_mul_f32 v201, v112, v201\n"
    "v_mul_f32 v202, v112, v202\n"
    "v_mul_f32 v203, v112, v203\n"
    "v_mfma_f32_16x16x32_bf16 a[136:139], v[96:99], v[40:43], a[136:139]\n"
    "ds_read_b64_tr_b16 v[84:85], v125 offset:51200\n"
    "v_cvt_pk_bf16_f32 v204, v200, v201\n"
    "v_cvt_pk_bf16_f32 v205, v202, v203\n"
    "v_mfma_f32_16x16x32_bf16 a[136:139], v[100:103], v[56:59], a[136:139]\n"
    "ds_write_b64 v251, v[204:205] offset:8192\n"
    "v_accvgpr_read_b32 v206, a116\n"
    "v_accvgpr_read_b32 v207, a117\n"
    "v_mfma_f32_16x16x32_bf16 a[140:143], v[100:103], v[60:63], a[140:143]\n"
    "ds_read_b64_tr_b16 v[86:87], v125 offset:59392\n"
    "buffer_store_dwordx4 v[244:247], v159, s[52:55], 0 offen offset:0 nt\n"
    "v_accvgpr_read_b32 v208, a118\n"
    "v_mfma_f32_16x16x32_bf16 a[140:143], v[96:99], v[44:47], a[140:143]\n"
    "ds_read_b128 v[184:187], v252 offset:8192\n"
    "v_accvgpr_read_b32 v209, a119\n"
    "v_mul_f32 v206, v113, v206\n"
    "s_waitcnt lgkmcnt(7)\n"
    "v_mfma_f32_16x16x32_bf16 a[144:147], v[104:107], v[128:131], a[144:147]\n"
    "ds_read_b64_tr_b16 v[88:89], v217 offset:2048\n"
    "v_mul_f32 v207, v113, v207\n"
    "v_mul_f32 v208, v113, v208\n"
    "v_mfma_f32_16x16x32_bf16 a[144:147], v[108:111], v[132:135], a[144:147]\n"
    "ds_read_b128 v[188:191], v252 offset:9216\n"
    "v_mul_f32 v209, v113, v209\n"
    "v_cvt_pk_bf16_f32 v210, v206, v207\n"
    "v_mfma_f32_16x16x32_bf16 a[148:151], v[108:111], v[164:167], a[148:151]\n"
    "ds_read_b64_tr_b16 v[90:91], v217 offset:10240\n"
    "v_cvt_pk_bf16_f32 v211, v208, v209\n"
    "v_accvgpr_read_b32 v136, a120\n"
    "v_mfma_f32_16x16x32_bf16 a[148:151], v[104:107], v[160:163], a[148:151]\n"
    "ds_write_b64 v251, v[210:211] offset:10240\n"
    "v_accvgpr_read_b32 v137, a121\n"
    "v_accvgpr_read_b32 v138, a122\n"
    "v_mfma_f32_16x16x32_bf16 a[152:155], v[104:107], v[192:195], a[152:155]\n"
    "ds_read_b64_tr_b16 v[92:93], v217 offset:18432\n"
    "v_accvgpr_read_b32 v139, a123\n"
    "v_mul_f32 v136, v114, v136\n"
    "v_mfma_f32_16x16x32_bf16 a[152:155], v[108:111], v[196:199], a[152:155]\n"
    "ds_read_b128 v[64:67], v252 offset:10240\n"
    "v_mul_f32 v137, v114, v137\n"
    "v_mul_f32 v138, v114, v138\n"
    "v_mfma_f32_16x16x32_bf16 a[156:159], v[108:111], v[228:231], a[156:159]\n"
    "ds_read_b64_tr_b16 v[94:95], v217 offset:26624\n"
    "v_mul_f32 v139, v114, v139\n"
    "v_cvt_pk_bf16_f32 v140, v136, v137\n"
    "v_mfma_f32_16x16x32_bf16 a[156:159], v[104:107], v[224:227], a[156:159]\n"
    "ds_read_b128 v[68:71], v252 offset:11264\n"
    "v_cvt_pk_bf16_f32 v141, v138, v139\n"
    "v_accvgpr_read_b32 v142, a124\n"
    "s_waitcnt lgkmcnt(9)\n"
    "v_mfma_f32_16x16x32_bf16 a[144:147], v[80:83], v[0:3], a[144:147]\n"
    "ds_read_b64_tr_b16 v[96:97], v126 offset:2048\n"
    "s_waitcnt lgkmcnt(7)\n"
    "buffer_store_dwordx4 v[184:187], v152, s[52:55], 0 offen offset:128 nt\n"
    "v_mfma_f32_16x16x32_bf16 a[144:147], v[84:87], v[16:19], a[144:147]\n"
    "ds_write_b64 v251, v[140:141] offset:12288\n"
    "v_accvgpr_read_b32 v143, a125\n"
    "v_accvgpr_read_b32 v144, a126\n"
    "v_mfma_f32_16x16x32_bf16 a[148:151], v[84:87], v[20:23], a[148:151]\n"
    "ds_read_b64_tr_b16 v[98:99], v126 offset:10240\n"
    "v_accvgpr_read_b32 v145, a127\n"
    "v_mul_f32 v142, v115, v142\n"
    "v_mfma_f32_16x16x32_bf16 a[148:151], v[80:83], v[4:7], a[148:151]\n"
    "ds_read_b128 v[72:75], v252 offset:12288\n"
    "v_mul_f32 v143, v115, v143\n"
    "v_mul_f32 v144, v115, v144\n"
    "v_mfma_f32_16x16x32_bf16 a[152:155], v[80:83], v[8:11], a[152:155]\n"
    "ds_read_b64_tr_b16 v[100:101], v126 offset:18432\n"
    "v_mul_f32 v145, v115, v145\n"
    "v_cvt_pk_bf16_f32 v146, v142, v143\n"
    "v_mfma_f32_16x16x32_bf16 a[152:155], v[84:87], v[24:27], a[152:155]\n"
    "ds_read_b128 v[76:79], v252 offset:13312\n"
    "v_cvt_pk_bf16_f32 v147, v144, v145\n"
    "v_accvgpr_read_b32 v168, a128\n"
    "v_mfma_f32_16x16x32_bf16 a[156:159], v[84:87], v[28:31], a[156:159]\n"
    "ds_read_b64_tr_b16 v[102:103], v126 offset:26624\n"
    "v_accvgpr_read_b32 v169, a129\n"
    "v_accvgpr_read_b32 v170, a130\n"
    "v_mfma_f32_16x16x32_bf16 a[156:159], v[80:83], v[12:15], a[156:159]\n"
    "ds_write_b64 v251, v[146:147] offset:14336\n"
    "v_accvgpr_read_b32 v171, a131\n"
    "v_mul_f32 v168, v112, v168\n"
    "s_waitcnt lgkmcnt(9)\n"
    "v_mfma_f32_16x16x32_bf16 a[144:147], v[88:91], v[32:35], a[144:147]\n"
    "ds_read_b64_tr_b16 v[104:105], v126 offset:34816\n"
    "v_mul_f32 v169, v112, v169\n"
    "v_mul_f32 v170, v112, v170\n"
    "v_mfma_f32_16x16x32_bf16 a[144:147], v[92:95], v[48:51], a[144:147]\n"
    "ds_read_b128 v[240:243], v252 offset:14336\n"
    "v_mul_f32 v171, v112, v171\n"
    "v_cvt_pk_bf16_f32 v172, v168, v169\n"
    "v_mfma_f32_16x16x32_bf16 a[148:151], v[92:95], v[52:55], a[148:151]\n"
    "ds_read_b64_tr_b16 v[106:107], v126 offset:43008\n"
    "buffer_store_dwordx4 v[188:191], v153, s[52:55], 0 offen offset:128 nt\n"
    "v_cvt_pk_bf16_f32 v173, v170, v171\n"
    "v_mfma_f32_16x16x32_bf16 a[148:151], v[88:91], v[36:39], a[148:151]\n"
    "ds_read_b128 v[244:247], v252 offset:15360\n"
    "v_accvgpr_read_b32 v174, a132\n"
    "v_accvgpr_read_b32 v175, a133\n"
    "v_mfma_f32_16x16x32_bf16 a[152:155], v[88:91], v[40:43], a[152:155]\n"
    "ds_read_b64_tr_b16 v[108:109], v126 offset:51200\n"
    "v_accvgpr_read_b32 v176, a134\n"
    "v_accvgpr_read_b32 v177, a135\n"
    "v_mfma_f32_16x16x32_bf16 a[152:155], v[92:95], v[56:59], a[152:155]\n"
    "ds_write_b64 v248, v[172:173] offset:0\n"
    "v_mul_f32 v174, v113, v174\n"
    "v_mul_f32 v175, v113, v175\n"
    "v_mfma_f32_16x16x32_bf16 a[156:159], v[92:95], v[60:63], a[156:159]\n"
    "ds_read_b64_tr_b16 v[110:111], v126 offset:59392\n"
    "v_mul_f32 v176, v113, v176\n"
    "v_mul_f32 v177, v113, v177\n"
    "v_mfma_f32_16x16x32_bf16 a[156:159], v[88:91], v[44:47], a[156:159]\n"
    "v_cvt_pk_bf16_f32 v178, v174, v175\n"
    "v_cvt_pk_bf16_f32 v179, v176, v177\n"
    "v_accvgpr_read_b32 v200, a136\n"
    "s_waitcnt lgkmcnt(8)\n"
    "v_mfma_f32_16x16x32_bf16 a[160:163], v[96:99], v[128:131], a[160:163]\n"
    "ds_read_b64_tr_b16 v[80:81], v218 offset:2048\n"
    "v_accvgpr_read_b32 v201, a137\n"
    "v_accvgpr_read_b32 v202, a138\n"
    "v_mfma_f32_16x16x32_bf16 a[160:163], v[100:103], v[132:135], a[160:163]\n"
    "ds_write_b64 v248, v[178:179] offset:2048\n"
    "v_accvgpr_read_b32 v203, a139\n"
    "v_mul_f32 v200, v114, v200\n"
    "v_mfma_f32_16x16x32_bf16 a[164:167], v[100:103], v[164:167], a[164:167]\n"
    "ds_read_b64_tr_b16 v[82:83], v218 offset:10240\n"
    "v_mul_f32 v201, v114, v201\n"
    "v_mul_f32 v202, v114, v202\n"
    "v_mfma_f32_16x16x32_bf16 a[164:167], v[96:99], v[160:163], a[164:167]\n"
    "v_mul_f32 v203, v114, v203\n"
    "v_cvt_pk_bf16_f32 v204, v200, v201\n"
    "v_cvt_pk_bf16_f32 v205, v202, v203\n"
    "v_mfma_f32_16x16x32_bf16 a[168:171], v[96:99], v[192:195], a[168:171]\n"
    "ds_read_b64_tr_b16 v[84:85], v218 offset:18432\n"
    "s_waitcnt lgkmcnt(15)\n"
    "buffer_store_dwordx4 v[64:67], v154, s[52:55], 0 offen offset:128 nt\n"
    "v_mfma_f32_16x16x32_bf16 a[168:171], v[100:103], v[196:199], a[168:171]\n"
    "ds_write_b64 v248, v[204:205] offset:4096\n"
    "v_accvgpr_read_b32 v206, a140\n"
    "v_accvgpr_read_b32 v207, a141\n"
    "v_mfma_f32_16x16x32_bf16 a[172:175], v[100:103], v[228:231], a[172:175]\n"
    "ds_read_b64_tr_b16 v[86:87], v218 offset:26624\n"
    "v_accvgpr_read_b32 v208, a142\n"
    "v_accvgpr_read_b32 v209, a143\n"
    "v_mfma_f32_16x16x32_bf16 a[172:175], v[96:99], v[224:227], a[172:175]\n"
    "v_mul_f32 v206, v115, v206\n"
    "v_mul_f32 v207, v115, v207\n"
    "v_mul_f32 v208, v115, v208\n"
    "s_waitcnt lgkmcnt(6)\n"
    "v_mfma_f32_16x16x32_bf16 a[160:163], v[104:107], v[0:3], a[160:163]\n"
    "ds_read_b64_tr_b16 v[88:89], v122 offset:2048\n"
    "v_mul_f32 v209, v115, v209\n"
    "v_cvt_pk_bf16_f32 v210, v206, v207\n"
    "v_mfma_f32_16x16x32_bf16 a[160:163], v[108:111], v[16:19], a[160:163]\n"
    "v_cvt_pk_bf16_f32 v211, v208, v209\n"
    "v_accvgpr_read_b32 v136, a144\n"
    "v_accvgpr_read_b32 v137, a145\n"
    "v_mfma_f32_16x16x32_bf16 a[164:167], v[108:111], v[20:23], a[164:167]\n"
    "ds_read_b64_tr_b16 v[90:91], v122 offset:10240\n"
    "v_accvgpr_read_b32 v138, a146\n"
    "v_accvgpr_read_b32 v139, a147\n"
    "v_mfma_f32_16x16x32_bf16 a[164:167], v[104:107], v[4:7], a[164:167]\n"
    "ds_write_b64 v248, v[210:211] offset:6144\n"
    "v_mul_f32 v136, v112, v136\n"
    "v_mul_f32 v137, v112, v137\n"
    "v_mfma_f32_16x16x32_bf16 a[168:171], v[104:107], v[8:11], a[168:171]\n"
    "ds_read_b64_tr_b16 v[92:93], v122 offset:18432\n"
    "v_mul_f32 v138, v112, v138\n"
    "v_mul_f32 v139, v112, v139\n"
    "v_mfma_f32_16x16x32_bf16 a[168:171], v[108:111], v[24:27], a[168:171]\n"
    "v_cvt_pk_bf16_f32 v140, v136, v137\n"
    "v_cvt_pk_bf16_f32 v141, v138, v139\n"
    "v_accvgpr_read_b32 v142, a148\n"
    "v_mfma_f32_16x16x32_bf16 a[172:175], v[108:111], v[28:31], a[172:175]\n"
    "ds_read_b64_tr_b16 v[94:95], v122 offset:26624\n"
    "buffer_store_dwordx4 v[68:71], v155, s[52:55], 0 offen offset:128 nt\n"
    "v_accvgpr_read_b32 v143, a149\n"
    "v_mfma_f32_16x16x32_bf16 a[172:175], v[104:107], v[12:15], a[172:175]\n"
    "ds_write_b64 v249, v[140:141] offset:0\n"
    "v_accvgpr_read_b32 v144, a150\n"
    "v_accvgpr_read_b32 v145, a151\n"
    "s_waitcnt lgkmcnt(6)\n"
    "v_mfma_f32_16x16x32_bf16 a[160:163], v[80:83], v[32:35], a[160:163]\n"
    "ds_read_b64_tr_b16 v[96:97], v122 offset:34816\n"
    "v_mul_f32 v142, v113, v142\n"
    "v_mul_f32 v143, v113, v143\n"
    "v_mfma_f32_16x16x32_bf16 a[160:163], v[84:87], v[48:51], a[160:163]\n"
    "v_mul_f32 v144, v113, v144\n"
    "v_mul_f32 v145, v113, v145\n"
    "v_cvt_pk_bf16_f32 v146, v142, v143\n"
    "v_mfma_f32_16x16x32_bf16 a[164:167], v[84:87], v[52:55], a[164:167]\n"
    "ds_read_b64_tr_b16 v[98:99], v122 offset:43008\n"
    "v_cvt_pk_bf16_f32 v147, v144, v145\n"
    "v_accvgpr_read_b32 v168, a152\n"
    "v_mfma_f32_16x16x32_bf16 a[164:167], v[80:83], v[36:39], a[164:167]\n"
    "ds_write_b64 v249, v[146:147] offset:2048\n"
    "v_accvgpr_read_b32 v169, a153\n"
    "v_accvgpr_read_b32 v170, a154\n"
    "v_mfma_f32_16x16x32_bf16 a[168:171], v[80:83], v[40:43], a[168:171]\n"
    "ds_read_b64_tr_b16 v[100:101], v122 offset:51200\n"
    "v_accvgpr_read_b32 v171, a155\n"
    "v_mul_f32 v168, v114, v168\n"
    "v_mfma_f32_16x16x32_bf16 a[168:171], v[84:87], v[56:59], a[168:171]\n"
    "v_mul_f32 v169, v114, v169\n"
    "v_mul_f32 v170, v114, v170\n"
    "v_mul_f32 v171, v114, v171\n"
    "v_mfma_f32_16x16x32_bf16 a[172:175], v[84:87], v[60:63], a[172:175]\n"
    "ds_read_b64_tr_b16 v[102:103], v122 offset:59392\n"
    "v_cvt_pk_bf16_f32 v172, v168, v169\n"
    "v_cvt_pk_bf16_f32 v173, v170, v171\n"
    "v_mfma_f32_16x16x32_bf16 a[172:175], v[80:83], v[44:47], a[172:175]\n"
    "ds_write_b64 v249, v[172:173] offset:4096\n"
    "v_accvgpr_read_b32 v174, a156\n"
    "v_accvgpr_read_b32 v175, a157\n"
    "s_waitcnt lgkmcnt(7)\n"
    "v_mfma_f32_16x16x32_bf16 a[176:179], v[88:91], v[128:131], a[176:179]\n"
    "ds_read_b64_tr_b16 v[104:105], v219 offset:2048\n"
    "s_waitcnt lgkmcnt(15)\n"
    "buffer_store_dwordx4 v[72:75], v156, s[52:55], 0 offen offset:128 nt\n"
    "v_mfma_f32_16x16x32_bf16 a[176:179], v[92:95], v[132:135], a[176:179]\n"
    "v_accvgpr_read_b32 v176, a158\n"
    "v_accvgpr_read_b32 v177, a159\n"
    "v_mul_f32 v174, v115, v174\n"
    "v_mfma_f32_16x16x32_bf16 a[180:183], v[92:95], v[164:167], a[180:183]\n"
    "ds_read_b64_tr_b16 v[106:107], v219 offset:10240\n"
    "v_mul_f32 v175, v115, v175\n"
    "v_mul_f32 v176, v115, v176\n"
    "v_mfma_f32_16x16x32_bf16 a[180:183], v[88:91], v[160:163], a[180:183]\n"
    "v_mul_f32 v177, v115, v177\n"
    "v_cvt_pk_bf16_f32 v178, v174, v175\n"
    "v_cvt_pk_bf16_f32 v179, v176, v177\n"
    "v_mfma_f32_16x16x32_bf16 a[184:187], v[88:91], v[192:195], a[184:187]\n"
    "ds_read_b64_tr_b16 v[108:109], v219 offset:18432\n"
    "v_accvgpr_read_b32 v200, a160\n"
    "v_accvgpr_read_b32 v201, a161\n"
    "v_mfma_f32_16x16x32_bf16 a[184:187], v[92:95], v[196:199], a[184:187]\n"
    "ds_write_b64 v249, v[178:179] offset:6144\n"
    "v_accvgpr_read_b32 v202, a162\n"
    "v_accvgpr_read_b32 v203, a163\n"
    "v_mfma_f32_16x16x32_bf16 a[188:191], v[92:95], v[228:231], a[188:191]\n"
    "ds_read_b64_tr_b16 v[110:111], v219 offset:26624\n"
    "v_mul_f32 v200, v112, v200\n"
    "v_mul_f32 v201, v112, v201\n"
    "v_mfma_f32_16x16x32_bf16 a[188:191], v[88:91], v[224:227], a[188:191]\n"
    "v_mul_f32 v202, v112, v202\n"
    "v_mul_f32 v203, v112, v203\n"
    "v_cvt_pk_bf16_f32 v204, v200, v201\n"
    "s_waitcnt lgkmcnt(6)\n"
    "v_mfma_f32_16x16x32_bf16 a[176:179], v[96:99], v[0:3], a[176:179]\n"
    "ds_read_b64_tr_b16 v[80:81], v124 offset:3072\n"
    "v_cvt_pk_bf16_f32 v205, v202, v203\n"
    "v_accvgpr_read_b32 v206, a164\n"
    "v_mfma_f32_16x16x32_bf16 a[176:179], v[100:103], v[16:19], a[176:179]\n"
    "ds_write_b64 v250, v[204:205] offset:0\n"
    "v_accvgpr_read_b32 v207, a165\n"
    "v_accvgpr_read_b32 v208, a166\n"
    "v_mfma_f32_16x16x32_bf16 a[180:183], v[100:103], v[20:23], a[180:183]\n"
    "ds_read_b64_tr_b16 v[82:83], v124 offset:11264\n"
    "buffer_store_dwordx4 v[76:79], v157, s[52:55], 0 offen offset:128 nt\n"
    "v_accvgpr_read_b32 v209, a167\n"
    "v_mfma_f32_16x16x32_bf16 a[180:183], v[96:99], v[4:7], a[180:183]\n"
    "v_mul_f32 v206, v113, v206\n"
    "v_mul_f32 v207, v113, v207\n"
    "v_mul_f32 v208, v113, v208\n"
    "v_mfma_f32_16x16x32_bf16 a[184:187], v[96:99], v[8:11], a[184:187]\n"
    "ds_read_b64_tr_b16 v[84:85], v124 offset:19456\n"
    "v_mul_f32 v209, v113, v209\n"
    "v_cvt_pk_bf16_f32 v210, v206, v207\n"
    "v_mfma_f32_16x16x32_bf16 a[184:187], v[100:103], v[24:27], a[184:187]\n"
    "v_cvt_pk_bf16_f32 v211, v208, v209\n"
    "v_accvgpr_read_b32 v136, a168\n"
    "v_accvgpr_read_b32 v137, a169\n"
    "v_mfma_f32_16x16x32_bf16 a[188:191], v[100:103], v[28:31], a[188:191]\n"
    "ds_read_b64_tr_b16 v[86:87], v124 offset:27648\n"
    "v_accvgpr_read_b32 v138, a170\n"
    "v_accvgpr_read_b32 v139, a171\n"
    "v_mfma_f32_16x16x32_bf16 a[188:191], v[96:99], v[12:15], a[188:191]\n"
    "ds_write_b64 v250, v[210:211] offset:2048\n"
    "v_mul_f32 v136, v114, v136\n"
    "v_mul_f32 v137, v114, v137\n"
    "s_waitcnt lgkmcnt(6)\n"
    "v_mfma_f32_16x16x32_bf16 a[176:179], v[104:107], v[32:35], a[176:179]\n"
    "ds_read_b64_tr_b16 v[88:89], v124 offset:35840\n"
    "v_mul_f32 v138, v114, v138\n"
    "v_mul_f32 v139, v114, v139\n"
    "v_mfma_f32_16x16x32_bf16 a[176:179], v[108:111], v[48:51], a[176:179]\n"
    "v_cvt_pk_bf16_f32 v140, v136, v137\n"
    "v_cvt_pk_bf16_f32 v141, v138, v139\n"
    "v_accvgpr_read_b32 v142, a172\n"
    "v_mfma_f32_16x16x32_bf16 a[180:183], v[108:111], v[52:55], a[180:183]\n"
    "ds_read_b64_tr_b16 v[90:91], v124 offset:44032\n"
    "v_accvgpr_read_b32 v143, a173\n"
    "v_accvgpr_read_b32 v144, a174\n"
    "v_mfma_f32_16x16x32_bf16 a[180:183], v[104:107], v[36:39], a[180:183]\n"
    "ds_write_b64 v250, v[140:141] offset:4096\n"
    "v_accvgpr_read_b32 v145, a175\n"
    "v_mul_f32 v142, v115, v142\n"
    "v_mfma_f32_16x16x32_bf16 a[184:187], v[104:107], v[40:43], a[184:187]\n"
    "ds_read_b64_tr_b16 v[92:93], v124 offset:52224\n"
    "s_waitcnt lgkmcnt(15)\n"
    "buffer_store_dwordx4 v[240:243], v158, s[52:55], 0 offen offset:128 nt\n"
    "v_mfma_f32_16x16x32_bf16 a[184:187], v[108:111], v[56:59], a[184:187]\n"
    "v_mul_f32 v143, v115, v143\n"
    "v_mul_f32 v144, v115, v144\n"
    "v_mul_f32 v145, v115, v145\n"
    "v_mfma_f32_16x16x32_bf16 a[188:191], v[108:111], v[60:63], a[188:191]\n"
    "ds_read_b64_tr_b16 v[94:95], v124 offset:60416\n"
    "v_cvt_pk_bf16_f32 v146, v142, v143\n"
    "v_cvt_pk_bf16_f32 v147, v144, v145\n"
    "v_mfma_f32_16x16x32_bf16 a[188:191], v[104:107], v[44:47], a[188:191]\n"
    "ds_write_b64 v250, v[146:147] offset:6144\n"
    "v_accvgpr_read_b32 v168, a176\n"
    "v_accvgpr_read_b32 v169, a177\n"
    "s_waitcnt lgkmcnt(7)\n"
    "v_mfma_f32_16x16x32_bf16 a[192:195], v[80:83], v[128:131], a[192:195]\n"
    "ds_read_b64_tr_b16 v[96:97], v216 offset:3072\n"
    "v_accvgpr_read_b32 v170, a178\n"
    "v_accvgpr_read_b32 v171, a179\n"
    "v_mfma_f32_16x16x32_bf16 a[192:195], v[84:87], v[132:135], a[192:195]\n"
    "v_mul_f32 v168, v112, v168\n"
    "v_mul_f32 v169, v112, v169\n"
    "v_mul_f32 v170, v112, v170\n"
    "v_mfma_f32_16x16x32_bf16 a[196:199], v[84:87], v[164:167], a[196:199]\n"
    "ds_read_b64_tr_b16 v[98:99], v216 offset:11264\n"
    "v_mul_f32 v171, v112, v171\n"
    "v_cvt_pk_bf16_f32 v172, v168, v169\n"
    "v_mfma_f32_16x16x32_bf16 a[196:199], v[80:83], v[160:163], a[196:199]\n"
    "v_cvt_pk_bf16_f32 v173, v170, v171\n"
    "v_accvgpr_read_b32 v174, a180\n"
    "v_accvgpr_read_b32 v175, a181\n"
    "v_mfma_f32_16x16x32_bf16 a[200:203], v[80:83], v[192:195], a[200:203]\n"
    "ds_read_b64_tr_b16 v[100:101], v216 offset:19456\n"
    "v_accvgpr_read_b32 v176, a182\n"
    "v_accvgpr_read_b32 v177, a183\n"
    "v_mfma_f32_16x16x32_bf16 a[200:203], v[84:87], v[196:199], a[200:203]\n"
    "ds_write_b64 v251, v[172:173] offset:0\n"
    "v_mul_f32 v174, v113, v174\n"
    "v_mul_f32 v175, v113, v175\n"
    "v_mfma_f32_16x16x32_bf16 a[204:207], v[84:87], v[228:231], a[204:207]\n"
    "ds_read_b64_tr_b16 v[102:103], v216 offset:27648\n"
    "buffer_store_dwordx4 v[244:247], v159, s[52:55], 0 offen offset:128 nt\n"
    "v_mul_f32 v176, v113, v176\n"
    "v_mfma_f32_16x16x32_bf16 a[204:207], v[80:83], v[224:227], a[204:207]\n"
    "ds_read_b128 v[184:187], v252 offset:0\n"
    "v_mul_f32 v177, v113, v177\n"
    "v_cvt_pk_bf16_f32 v178, v174, v175\n"
    "s_waitcnt lgkmcnt(7)\n"
    "v_mfma_f32_16x16x32_bf16 a[192:195], v[88:91], v[0:3], a[192:195]\n"
    "ds_read_b64_tr_b16 v[104:105], v125 offset:3072\n"
    "v_cvt_pk_bf16_f32 v179, v176, v177\n"
    "v_accvgpr_read_b32 v200, a184\n"
    "v_mfma_f32_16x16x32_bf16 a[192:195], v[92:95], v[16:19], a[192:195]\n"
    "ds_read_b128 v[188:191], v252 offset:1024\n"
    "v_accvgpr_read_b32 v201, a185\n"
    "v_accvgpr_read_b32 v202, a186\n"
    "v_mfma_f32_16x16x32_bf16 a[196:199], v[92:95], v[20:23], a[196:199]\n"
    "ds_read_b64_tr_b16 v[106:107], v125 offset:11264\n"
    "v_accvgpr_read_b32 v203, a187\n"
    "v_mul_f32 v200, v114, v200\n"
    "v_mfma_f32_16x16x32_bf16 a[196:199], v[88:91], v[4:7], a[196:199]\n"
    "ds_write_b64 v251, v[178:179] offset:2048\n"
    "v_mul_f32 v201, v114, v201\n"
    "v_mul_f32 v202, v114, v202\n"
    "v_mfma_f32_16x16x32_bf16 a[200:203], v[88:91], v[8:11], a[200:203]\n"
    "ds_read_b64_tr_b16 v[108:109], v125 offset:19456\n"
    "v_mul_f32 v203, v114, v203\n"
    "v_cvt_pk_bf16_f32 v204, v200, v201\n"
    "v_mfma_f32_16x16x32_bf16 a[200:203], v[92:95], v[24:27], a[200:203]\n"
    "ds_read_b128 v[64:67], v252 offset:2048\n"
    "v_cvt_pk_bf16_f32 v205, v202, v203\n"
    "v_accvgpr_read_b32 v206, a188\n"
    "v_mfma_f32_16x16x32_bf16 a[204:207], v[92:95], v[28:31], a[204:207]\n"
    "ds_read_b64_tr_b16 v[110:111], v125 offset:27648\n"
    "ds_read_b128 v[68:71], v252 offset:3072\n"
    "v_accvgpr_read_b32 v207, a189\n"
    "v_mfma_f32_16x16x32_bf16 a[204:207], v[88:91], v[12:15], a[204:207]\n"
    "ds_write_b64 v251, v[204:205] offset:4096\n"
    "v_accvgpr_read_b32 v208, a190\n"
    "v_accvgpr_read_b32 v209, a191\n"
    "s_waitcnt lgkmcnt(10)\n"
    "v_mfma_f32_16x16x32_bf16 a[192:195], v[96:99], v[32:35], a[192:195]\n"
    "ds_read_b64_tr_b16 v[80:81], v125 offset:35840\n"
    "s_waitcnt lgkmcnt(8)\n"
    "buffer_store_dwordx4 v[184:187], v152, s[52:55], 0 offen offset:256 nt\n"
    "v_mfma_f32_16x16x32_bf16 a[192:195], v[100:103], v[48:51], a[192:195]\n"
    "ds_read_b128 v[72:75], v252 offset:4096\n"
    "v_mul_f32 v206, v115, v206\n"
    "v_mul_f32 v207, v115, v207\n"
    "v_mfma_f32_16x16x32_bf16 a[196:199], v[100:103], v[52:55], a[196:199]\n"
    "ds_read_b64_tr_b16 v[82:83], v125 offset:44032\n"
    "v_mul_f32 v208, v115, v208\n"
    "v_mul_f32 v209, v115, v209\n"
    "v_mfma_f32_16x16x32_bf16 a[196:199], v[96:99], v[36:39], a[196:199]\n"
    "ds_read_b128 v[76:79], v252 offset:5120\n"
    "v_cvt_pk_bf16_f32 v210, v206, v207\n"
    "v_cvt_pk_bf16_f32 v211, v208, v209\n"
    "v_mfma_f32_16x16x32_bf16 a[200:203], v[96:99], v[40:43], a[200:203]\n"
    "ds_read_b64_tr_b16 v[84:85], v125 offset:52224\n"
    "s_mov_b32 s67, 0x2b800000\n"
    "s_mov_b32 s68, 0x53800000\n"
    "v_mfma_f32_16x16x32_bf16 a[200:203], v[100:103], v[56:59], a[200:203]\n"
    "ds_write_b64 v251, v[210:211] offset:6144\n"
    "v_cmp_nle_f32 s[70:71], s67, v116\n"
    "v_cmp_nge_f32 s[72:73], s68, v116\n"
    "v_mfma_f32_16x16x32_bf16 a[204:207], v[100:103], v[60:63], a[204:207]\n"
    "ds_read_b64_tr_b16 v[86:87], v125 offset:60416\n"
    "v_log_f32 v117, v116\n"
    "v_mfma_f32_16x16x32_bf16 a[204:207], v[96:99], v[44:47], a[204:207]\n"
    "ds_read_b128 v[240:243], v252 offset:6144\n"
    "s_mov_b32 s68, 0x42300000\n"
    "v_sub_f32 v117, v117, v119\n"
    "s_waitcnt lgkmcnt(10)\n"
    "v_mfma_f32_16x16x32_bf16 a[208:211], v[104:107], v[128:131], a[208:211]\n"
    "ds_read_b64_tr_b16 v[88:89], v217 offset:3072\n"
    "s_or_b64 s[70:71], s[70:71], s[72:73]\n"
    "v_cmp_nge_f32 s[72:73], s68, |v117|\n"
    "v_mfma_f32_16x16x32_bf16 a[208:211], v[108:111], v[132:135], a[208:211]\n"
    "ds_read_b128 v[244:247], v252 offset:7168\n"
    "s_cmp_ge_u32 s21, 0x38000\n"
    "s_cselect_b32 s67, 0xffff, -1\n"
    "v_mfma_f32_16x16x32_bf16 a[212:215], v[108:111], v[164:167], a[212:215]\n"
    "ds_read_b64_tr_b16 v[90:91], v217 offset:11264\n"
    "buffer_store_dwordx4 v[188:191], v153, s[52:55], 0 offen offset:256 nt\n"
    "v_accvgpr_read_b32 v136, a192\n"
    "v_mfma_f32_16x16x32_bf16 a[212:215], v[104:107], v[160:163], a[212:215]\n"
    "v_accvgpr_read_b32 v137, a193\n"
    "v_accvgpr_read_b32 v138, a194\n"
    "v_accvgpr_read_b32 v139, a195\n"
    "v_mfma_f32_16x16x32_bf16 a[216:219], v[104:107], v[192:195], a[216:219]\n"
    "ds_read_b64_tr_b16 v[92:93], v217 offset:19456\n"
    "v_mul_f32 v136, v112, v136\n"
    "v_mul_f32 v137, v112, v137\n"
    "v_mfma_f32_16x16x32_bf16 a[216:219], v[108:111], v[196:199], a[216:219]\n"
    "v_mul_f32 v138, v112, v138\n"
    "v_mul_f32 v139, v112, v139\n"
    "v_cvt_pk_bf16_f32 v140, v136, v137\n"
    "v_mfma_f32_16x16x32_bf16 a[220:223], v[108:111], v[228:231], a[220:223]\n"
    "ds_read_b64_tr_b16 v[94:95], v217 offset:27648\n"
    "v_cvt_pk_bf16_f32 v141, v138, v139\n"
    "v_accvgpr_read_b32 v142, a196\n"
    "v_mfma_f32_16x16x32_bf16 a[220:223], v[104:107], v[224:227], a[220:223]\n"
    "ds_write_b64 v248, v[140:141] offset:8192\n"
    "v_accvgpr_read_b32 v143, a197\n"
    "v_accvgpr_read_b32 v144, a198\n"
    "s_waitcnt lgkmcnt(7)\n"
    "v_mfma_f32_16x16x32_bf16 a[208:211], v[80:83], v[0:3], a[208:211]\n"
    "ds_read_b64_tr_b16 v[96:97], v126 offset:3072\n"
    "v_accvgpr_read_b32 v145, a199\n"
    "v_mul_f32 v142, v113, v142\n"
    "v_mfma_f32_16x16x32_bf16 a[208:211], v[84:87], v[16:19], a[208:211]\n"
    "v_mul_f32 v143, v113, v143\n"
    "v_mul_f32 v144, v113, v144\n"
    "v_mul_f32 v145, v113, v145\n"
    "v_mfma_f32_16x16x32_bf16 a[212:215], v[84:87], v[20:23], a[212:215]\n"
    "ds_read_b64_tr_b16 v[98:99], v126 offset:11264\n"
    "v_cvt_pk_bf16_f32 v146, v142, v143\n"
    "v_cvt_pk_bf16_f32 v147, v144, v145\n"
    "v_mfma_f32_16x16x32_bf16 a[212:215], v[80:83], v[4:7], a[212:215]\n"
    "ds_write_b64 v248, v[146:147] offset:10240\n"
    "v_accvgpr_read_b32 v168, a200\n"
    "v_accvgpr_read_b32 v169, a201\n"
    "v_mfma_f32_16x16x32_bf16 a[216:219], v[80:83], v[8:11], a[216:219]\n"
    "ds_read_b64_tr_b16 v[100:101], v126 offset:19456\n"
    "s_waitcnt lgkmcnt(15)\n"
    "buffer_store_dwordx4 v[64:67], v154, s[52:55], 0 offen offset:256 nt\n"
    "v_mfma_f32_16x16x32_bf16 a[216:219], v[84:87], v[24:27], a[216:219]\n"
    "v_accvgpr_read_b32 v170, a202\n"
    "v_accvgpr_read_b32 v171, a203\n"
    "v_mul_f32 v168, v114, v168\n"
    "v_mfma_f32_16x16x32_bf16 a[220:223], v[84:87], v[28:31], a[220:223]\n"
    "ds_read_b64_tr_b16 v[102:103], v126 offset:27648\n"
    "v_mul_f32 v169, v114, v169\n"
    "v_mul_f32 v170, v114, v170\n"
    "v_mfma_f32_16x16x32_bf16 a[220:223], v[80:83], v[12:15], a[220:223]\n"
    "v_mul_f32 v171, v114, v171\n"
    "v_cvt_pk_bf16_f32 v172, v168, v169\n"
    "v_cvt_pk_bf16_f32 v173, v170, v171\n"
    "s_waitcnt lgkmcnt(6)\n"
    "v_mfma_f32_16x16x32_bf16 a[208:211], v[88:91], v[32:35], a[208:211]\n"
    "ds_read_b64_tr_b16 v[104:105], v126 offset:35840\n"
    "v_accvgpr_read_b32 v174, a204\n"
    "v_accvgpr_read_b32 v175, a205\n"
    "v_mfma_f32_16x16x32_bf16 a[208:211], v[92:95], v[48:51], a[208:211]\n"
    "ds_write_b64 v248, v[172:173] offset:12288\n"
    "v_accvgpr_read_b32 v176, a206\n"
    "v_accvgpr_read_b32 v177, a207\n"
    "v_mfma_f32_16x16x32_bf16 a[212:215], v[92:95], v[52:55], a[212:215]\n"
    "ds_read_b64_tr_b16 v[106:107], v126 offset:44032\n"
    "v_mul_f32 v174, v115, v174\n"
    "v_mul_f32 v175, v115, v175\n"
    "v_mfma_f32_16x16x32_bf16 a[212:215], v[88:91], v[36:39], a[212:215]\n"
    "v_mul_f32 v176, v115, v176\n"
    "v_mul_f32 v177, v115, v177\n"
    "v_cvt_pk_bf16_f32 v178, v174, v175\n"
    "v_mfma_f32_16x16x32_bf16 a[216:219], v[88:91], v[40:43], a[216:219]\n"
    "ds_read_b64_tr_b16 v[108:109], v126 offset:52224\n"
    "v_cvt_pk_bf16_f32 v179, v176, v177\n"
    "v_mfma_f32_16x16x32_bf16 a[216:219], v[92:95], v[56:59], a[216:219]\n"
    "ds_write_b64 v248, v[178:179] offset:14336\n"
    "v_accvgpr_read_b32 v200, a208\n"
    "v_accvgpr_read_b32 v201, a209\n"
    "v_mfma_f32_16x16x32_bf16 a[220:223], v[92:95], v[60:63], a[220:223]\n"
    "ds_read_b64_tr_b16 v[110:111], v126 offset:60416\n"
    "buffer_store_dwordx4 v[68:71], v155, s[52:55], 0 offen offset:256 nt\n"
    "v_accvgpr_read_b32 v202, a210\n"
    "v_mfma_f32_16x16x32_bf16 a[220:223], v[88:91], v[44:47], a[220:223]\n"
    "v_accvgpr_read_b32 v203, a211\n"
    "v_mul_f32 v200, v112, v200\n"
    "v_mul_f32 v201, v112, v201\n"
    "s_waitcnt lgkmcnt(6)\n"
    "v_mfma_f32_16x16x32_bf16 a[224:227], v[96:99], v[128:131], a[224:227]\n"
    "ds_read_b64_tr_b16 v[80:81], v218 offset:3072\n"
    "v_mul_f32 v202, v112, v202\n"
    "v_mul_f32 v203, v112, v203\n"
    "v_mfma_f32_16x16x32_bf16 a[224:227], v[100:103], v[132:135], a[224:227]\n"
    "v_cvt_pk_bf16_f32 v204, v200, v201\n"
    "v_cvt_pk_bf16_f32 v205, v202, v203\n"
    "v_accvgpr_read_b32 v206, a212\n"
    "v_mfma_f32_16x16x32_bf16 a[228:231], v[100:103], v[164:167], a[228:231]\n"
    "ds_read_b64_tr_b16 v[82:83], v218 offset:11264\n"
    "v_accvgpr_read_b32 v207, a213\n"
    "v_accvgpr_read_b32 v208, a214\n"
    "v_mfma_f32_16x16x32_bf16 a[228:231], v[96:99], v[160:163], a[228:231]\n"
    "ds_write_b64 v249, v[204:205] offset:8192\n"
    "v_accvgpr_read_b32 v209, a215\n"
    "v_mul_f32 v206, v113, v206\n"
    "v_mfma_f32_16x16x32_bf16 a[232:235], v[96:99], v[192:195], a[232:235]\n"
    "ds_read_b64_tr_b16 v[84:85], v218 offset:19456\n"
    "v_mul_f32 v207, v113, v207\n"
    "v_mul_f32 v208, v113, v208\n"
    "v_mfma_f32_16x16x32_bf16 a[232:235], v[100:103], v[196:199], a[232:235]\n"
    "v_mul_f32 v209, v113, v209\n"
    "v_cvt_pk_bf16_f32 v210, v206, v207\n"
    "v_cvt_pk_bf16_f32 v211, v208, v209\n"
    "v_mfma_f32_16x16x32_bf16 a[236:239], v[100:103], v[228:231], a[236:239]\n"
    "ds_read_b64_tr_b16 v[86:87], v218 offset:27648\n"
    "v_accvgpr_read_b32 v136, a216\n"
    "v_accvgpr_read_b32 v137, a217\n"
    "v_mfma_f32_16x16x32_bf16 a[236:239], v[96:99], v[224:227], a[236:239]\n"
    "ds_write_b64 v249, v[210:211] offset:10240\n"
    "v_accvgpr_read_b32 v138, a218\n"
    "v_accvgpr_read_b32 v139, a219\n"
    "s_waitcnt lgkmcnt(6)\n"
    "v_mfma_f32_16x16x32_bf16 a[224:227], v[104:107], v[0:3], a[224:227]\n"
    "ds_read_b64_tr_b16 v[88:89], v122 offset:3072\n"
    "s_waitcnt lgkmcnt(15)\n"
    "buffer_store_dwordx4 v[72:75], v156, s[52:55], 0 offen offset:256 nt\n"
    "v_mfma_f32_16x16x32_bf16 a[224:227], v[108:111], v[16:19], a[224:227]\n"
    "v_mul_f32 v136, v114, v136\n"
    "v_mul_f32 v137, v114, v137\n"
    "v_mul_f32 v138, v114, v138\n"
    "v_mfma_f32_16x16x32_bf16 a[228:231], v[108:111], v[20:23], a[228:231]\n"
    "ds_read_b64_tr_b16 v[90:91], v122 offset:11264\n"
    "v_mul_f32 v139, v114, v139\n"
    "v_cvt_pk_bf16_f32 v140, v136, v137\n"
    "v_mfma_f32_16x16x32_bf16 a[228:231], v[104:107], v[4:7], a[228:231]\n"
    "v_cvt_pk_bf16_f32 v141, v138, v139\n"
    "v_accvgpr_read_b32 v142, a220\n"
    "v_accvgpr_read_b32 v143, a221\n"
    "v_mfma_f32_16x16x32_bf16 a[232:235], v[104:107], v[8:11], a[232:235]\n"
    "ds_read_b64_tr_b16 v[92:93], v122 offset:19456\n"
    "v_accvgpr_read_b32 v144, a222\n"
    "v_accvgpr_read_b32 v145, a223\n"
    "v_mfma_f32_16x16x32_bf16 a[232:235], v[108:111], v[24:27], a[232:235]\n"
    "ds_write_b64 v249, v[140:141] offset:12288\n"
    "v_mul_f32 v142, v115, v142\n"
    "v_mul_f32 v143, v115, v143\n"
    "v_mfma_f32_16x16x32_bf16 a[236:239], v[108:111], v[28:31], a[236:239]\n"
    "ds_read_b64_tr_b16 v[94:95], v122 offset:27648\n"
    "v_mul_f32 v144, v115, v144\n"
    "v_mul_f32 v145, v115, v145\n"
    "v_mfma_f32_16x16x32_bf16 a[236:239], v[104:107], v[12:15], a[236:239]\n"
    "v_cvt_pk_bf16_f32 v146, v142, v143\n"
    "v_cvt_pk_bf16_f32 v147, v144, v145\n"
    "s_waitcnt lgkmcnt(6)\n"
    "v_mfma_f32_16x16x32_bf16 a[224:227], v[80:83], v[32:35], a[224:227]\n"
    "ds_read_b64_tr_b16 v[96:97], v122 offset:35840\n"
    "v_mfma_f32_16x16x32_bf16 a[224:227], v[84:87], v[48:51], a[224:227]\n"
    "ds_read_b64_tr_b16 v[98:99], v122 offset:44032\n"
    "v_mfma_f32_16x16x32_bf16 a[228:231], v[84:87], v[52:55], a[228:231]\n"
    "ds_read_b64_tr_b16 v[100:101], v122 offset:52224\n"
    "buffer_store_dwordx4 v[76:79], v157, s[52:55], 0 offen offset:256 nt\n"
    "v_mfma_f32_16x16x32_bf16 a[228:231], v[80:83], v[36:39], a[228:231]\n"
    "ds_read_b64_tr_b16 v[102:103], v122 offset:60416\n"
    "v_mfma_f32_16x16x32_bf16 a[232:235], v[80:83], v[40:43], a[232:235]\n"
    "ds_read_b64_tr_b16 v[104:105], v219 offset:3072\n"
    "v_mfma_f32_16x16x32_bf16 a[232:235], v[84:87], v[56:59], a[232:235]\n"
    "ds_read_b64_tr_b16 v[106:107], v219 offset:11264\n"
    "v_accvgpr_read_b32 v168, a224\n"
    "v_accvgpr_read_b32 v169, a225\n"
    "v_mfma_f32_16x16x32_bf16 a[236:239], v[84:87], v[60:63], a[236:239]\n"
    "ds_read_b64_tr_b16 v[108:109], v219 offset:19456\n"
    "v_accvgpr_read_b32 v170, a226\n"
    "v_accvgpr_read_b32 v171, a227\n"
    "v_mfma_f32_16x16x32_bf16 a[236:239], v[80:83], v[44:47], a[236:239]\n"
    "ds_read_b64_tr_b16 v[110:111], v219 offset:27648\n"
    "v_mul_f32 v168, v112, v168\n"
    "v_mul_f32 v169, v112, v169\n"
    "s_waitcnt lgkmcnt(4)\n"
    "v_mfma_f32_16x16x32_bf16 a[240:243], v[88:91], v[128:131], a[240:243]\n"
    "ds_write_b64 v249, v[146:147] offset:14336\n"
    "v_mul_f32 v170, v112, v170\n"
    "v_mul_f32 v171, v112, v171\n"
    "v_mfma_f32_16x16x32_bf16 a[240:243], v[92:95], v[132:135], a[240:243]\n"
    "v_cvt_pk_bf16_f32 v172, v168, v169\n"
    "v_cvt_pk_bf16_f32 v173, v170, v171\n"
    "v_accvgpr_read_b32 v174, a228\n"
    "v_mfma_f32_16x16x32_bf16 a[240:243], v[96:99], v[0:3], a[240:243]\n"
    "ds_write_b64 v250, v[172:173] offset:8192\n"
    "v_accvgpr_read_b32 v175, a229\n"
    "v_accvgpr_read_b32 v176, a230\n"
    "v_mfma_f32_16x16x32_bf16 a[240:243], v[100:103], v[16:19], a[240:243]\n"
    "v_accvgpr_read_b32 v177, a231\n"
    "v_mul_f32 v174, v113, v174\n"
    "v_mul_f32 v175, v113, v175\n"
    "s_waitcnt lgkmcnt(2)\n"
    "v_mfma_f32_16x16x32_bf16 a[240:243], v[104:107], v[32:35], a[240:243]\n"
    "s_waitcnt lgkmcnt(15)\n"
    "buffer_store_dwordx4 v[240:243], v158, s[52:55], 0 offen offset:256 nt\n"
    "v_mul_f32 v176, v113, v176\n"
    "v_mfma_f32_16x16x32_bf16 a[240:243], v[108:111], v[48:51], a[240:243]\n"
    "v_mul_f32 v177, v113, v177\n"
    "v_cvt_pk_bf16_f32 v178, v174, v175\n"
    "v_cvt_pk_bf16_f32 v179, v176, v177\n"
    "v_mfma_f32_16x16x32_bf16 a[244:247], v[92:95], v[164:167], a[244:247]\n"
    "ds_write_b64 v250, v[178:179] offset:10240\n"
    "v_accvgpr_read_b32 v200, a232\n"
    "v_accvgpr_read_b32 v201, a233\n"
    "v_mfma_f32_16x16x32_bf16 a[244:247], v[88:91], v[160:163], a[244:247]\n"
    "v_accvgpr_read_b32 v202, a234\n"
    "v_accvgpr_read_b32 v203, a235\n"
    "v_mul_f32 v200, v114, v200\n"
    "v_mfma_f32_16x16x32_bf16 a[244:247], v[100:103], v[20:23], a[244:247]\n"
    "v_mul_f32 v201, v114, v201\n"
    "v_mul_f32 v202, v114, v202\n"
    "v_mul_f32 v203, v114, v203\n"
    "v_mfma_f32_16x16x32_bf16 a[244:247], v[96:99], v[4:7], a[244:247]\n"
    "v_cvt_pk_bf16_f32 v204, v200, v201\n"
    "v_cvt_pk_bf16_f32 v205, v202, v203\n"
    "v_accvgpr_read_b32 v206, a236\n"
    "v_mfma_f32_16x16x32_bf16 a[244:247], v[108:111], v[52:55], a[244:247]\n"
    "ds_write_b64 v250, v[204:205] offset:12288\n"
    "v_accvgpr_read_b32 v207, a237\n"
    "v_accvgpr_read_b32 v208, a238\n"
    "v_mfma_f32_16x16x32_bf16 a[244:247], v[104:107], v[36:39], a[244:247]\n"
    "v_accvgpr_read_b32 v209, a239\n"
    "v_mul_f32 v206, v115, v206\n"
    "v_mul_f32 v207, v115, v207\n"
    "v_mfma_f32_16x16x32_bf16 a[248:251], v[88:91], v[192:195], a[248:251]\n"
    "v_mul_f32 v208, v115, v208\n"
    "v_mul_f32 v209, v115, v209\n"
    "v_cvt_pk_bf16_f32 v210, v206, v207\n"
    "v_mfma_f32_16x16x32_bf16 a[248:251], v[92:95], v[196:199], a[248:251]\n"
    "v_cvt_pk_bf16_f32 v211, v208, v209\n"
    "v_accvgpr_read_b32 v136, a240\n"
    "v_accvgpr_read_b32 v137, a241\n"
    "v_mfma_f32_16x16x32_bf16 a[248:251], v[96:99], v[8:11], a[248:251]\n"
    "ds_write_b64 v250, v[210:211] offset:14336\n"
    "buffer_store_dwordx4 v[244:247], v159, s[52:55], 0 offen offset:256 nt\n"
    "v_accvgpr_read_b32 v138, a242\n"
    "v_mfma_f32_16x16x32_bf16 a[248:251], v[100:103], v[24:27], a[248:251]\n"
    "v_accvgpr_read_b32 v139, a243\n"
    "v_mul_f32 v136, v112, v136\n"
    "v_mul_f32 v137, v112, v137\n"
    "v_mfma_f32_16x16x32_bf16 a[248:251], v[104:107], v[40:43], a[248:251]\n"
    "v_mul_f32 v138, v112, v138\n"
    "v_mul_f32 v139, v112, v139\n"
    "v_cvt_pk_bf16_f32 v140, v136, v137\n"
    "v_mfma_f32_16x16x32_bf16 a[248:251], v[108:111], v[56:59], a[248:251]\n"
    "v_cvt_pk_bf16_f32 v141, v138, v139\n"
    "v_accvgpr_read_b32 v142, a244\n"
    "v_accvgpr_read_b32 v143, a245\n"
    "v_mfma_f32_16x16x32_bf16 a[252:255], v[92:95], v[228:231], a[252:255]\n"
    "ds_write_b64 v251, v[140:141] offset:8192\n"
    "v_accvgpr_read_b32 v144, a246\n"
    "v_accvgpr_read_b32 v145, a247\n"
    "v_mfma_f32_16x16x32_bf16 a[252:255], v[88:91], v[224:227], a[252:255]\n"
    "ds_read_b128 v[184:187], v252 offset:8192\n"
    "v_mul_f32 v142, v113, v142\n"
    "v_mul_f32 v143, v113, v143\n"
    "v_mfma_f32_16x16x32_bf16 a[252:255], v[100:103], v[28:31], a[252:255]\n"
    "ds_read_b128 v[188:191], v252 offset:9216\n"
    "v_mul_f32 v144, v113, v144\n"
    "v_mul_f32 v145, v113, v145\n"
    "v_mfma_f32_16x16x32_bf16 a[252:255], v[96:99], v[12:15], a[252:255]\n"
    "v_cvt_pk_bf16_f32 v146, v142, v143\n"
    "v_cvt_pk_bf16_f32 v147, v144, v145\n"
    "v_accvgpr_read_b32 v168, a248\n"
    "v_mfma_f32_16x16x32_bf16 a[252:255], v[108:111], v[60:63], a[252:255]\n"
    "ds_write_b64 v251, v[146:147] offset:10240\n"
    "v_accvgpr_read_b32 v169, a249\n"
    "v_accvgpr_read_b32 v170, a250\n"
    "v_mfma_f32_16x16x32_bf16 a[252:255], v[104:107], v[44:47], a[252:255]\n"
    "ds_read_b128 v[64:67], v252 offset:10240\n"
    "v_accvgpr_read_b32 v171, a251\n"
    "v_mul_f32 v168, v114, v168\n"
    "v_mul_f32 v169, v114, v169\n"
    "v_mul_f32 v170, v114, v170\n"
    "v_mul_f32 v171, v114, v171\n"
    "v_cvt_pk_bf16_f32 v172, v168, v169\n"
    "v_cvt_pk_bf16_f32 v173, v170, v171\n"
    "ds_read_b128 v[68:71], v252 offset:11264\n"
    "ds_write_b64 v251, v[172:173] offset:12288\n"
    "ds_read_b128 v[72:75], v252 offset:12288\n"
    "ds_read_b128 v[76:79], v252 offset:13312\n"
    "v_accvgpr_read_b32 v174, a252\n"
    "v_accvgpr_read_b32 v175, a253\n"
    "v_accvgpr_read_b32 v176, a254\n"
    "v_accvgpr_read_b32 v177, a255\n"
    "v_mul_f32 v174, v115, v174\n"
    "v_mul_f32 v175, v115, v175\n"
    "v_mul_f32 v176, v115, v176\n"
    "v_mul_f32 v177, v115, v177\n"
    "v_cvt_pk_bf16_f32 v178, v174, v175\n"
    "v_cvt_pk_bf16_f32 v179, v176, v177\n"
    "s_waitcnt lgkmcnt(6)\n"
    "buffer_store_dwordx4 v[184:187], v152, s[52:55], 0 offen offset:384 nt\n"
    "buffer_store_dwordx4 v[188:191], v153, s[52:55], 0 offen offset:384 nt\n"
    "ds_write_b64 v251, v[178:179] offset:14336\n"
    "ds_read_b128 v[240:243], v252 offset:14336\n"
    "ds_read_b128 v[244:247], v252 offset:15360\n"
    "s_waitcnt lgkmcnt(6)\n"
    "buffer_store_dwordx4 v[64:67], v154, s[52:55], 0 offen offset:384 nt\n"
    "buffer_store_dwordx4 v[68:71], v155, s[52:55], 0 offen offset:384 nt\n"
    "s_waitcnt lgkmcnt(3)\n"
    "buffer_store_dwordx4 v[72:75], v156, s[52:55], 0 offen offset:384 nt\n"
    "buffer_store_dwordx4 v[76:79], v157, s[52:55], 0 offen offset:384 nt\n"
    "s_waitcnt lgkmcnt(0)\n"
    "buffer_store_dwordx4 v[240:243], v158, s[52:55], 0 offen offset:384 nt\n"
    "buffer_store_dwordx4 v[244:247], v159, s[52:55], 0 offen offset:384 nt\n"
    "LEND%=:\n"
    "s_or_b64 s[70:71], s[70:71], s[72:73]\n"
    "s_and_b32 s71, s71, s67\n"
    "s_or_b32 %[flag], s70, s71\n"
    : [flag] "=s"(flag)
    : [ka] "s"(ka), [bid] "s"(bid), [tid] "s"(tid), [lb] "s"(lb)
    : "memory", "vcc", "scc", "v0","v1","v2","v3","v4","v5","v6","v7","v8","v9","v10","v11","v12","v13","v14","v15","v16","v17","v18","v19","v20","v21","v22","v23","v24","v25","v26","v27","v28","v29","v30","v31","v32","v33","v34","v35","v36","v37","v38","v39","v40","v41","v42","v43","v44","v45","v46","v47","v48","v49","v50","v51","v52","v53","v54","v55","v56","v57","v58","v59","v60","v61","v62","v63","v64","v65","v66","v67","v68","v69","v70","v71","v72","v73","v74","v75","v76","v77","v78","v79","v80","v81","v82","v83","v84","v85","v86","v87","v88","v89","v90","v91","v92","v93","v94","v95","v96","v97","v98","v99","v100","v101","v102","v103","v104","v105","v106","v107","v108","v109","v110","v111","v112","v113","v114","v115","v116","v117","v118","v119","v120","v121","v122","v123","v124","v125","v126","v127","v128","v129","v130","v131","v132","v133","v134","v135","v136","v137","v138","v139","v140","v141","v142","v143","v144","v145","v146","v147","v148","v149","v150","v151","v152","v153","v154","v155","v156","v157","v158","v159","v160","v161","v162","v163","v164","v165","v166","v167","v168","v169","v170","v171","v172","v173","v174","v175","v176","v177","v178","v179","v180","v181","v182","v183","v184","v185","v186","v187","v188","v189","v190","v191","v192","v193","v194","v195","v196","v197","v198","v199","v200","v201","v202","v203","v204","v205","v206","v207","v208","v209","v210","v211","v212","v213","v214","v215","v216","v217","v218","v219","v220","v221","v222","v223","v224","v225","v226","v227","v228","v229","v230","v231","v232","v233","v234","v235","v236","v237","v238","v239","v240","v241","v242","v243","v244","v245","v246","v247","v248","v249","v250","v251","v252","v253","v254","v255","a0","a1","a2","a3","a4","a5","a6","a7","a8","a9","a10","a11","a12","a13","a14","a15","a16","a17","a18","a19","a20","a21","a22","a23","a24","a25","a26","a27","a28","a29","a30","a31","a32","a33","a34","a35","a36","a37","a38","a39","a40","a41","a42","a43","a44","a45","a46","a47","a48","a49","a50","a51","a52","a53","a54","a55","a56","a57","a58","a59","a60","a61","a62","a63","a64","a65","a66","a67","a68","a69","a70","a71","a72","a73","a74","a75","a76","a77","a78","a79","a80","a81","a82","a83","a84","a85","a86","a87","a88","a89","a90","a91","a92","a93","a94","a95","a96","a97","a98","a99","a100","a101","a102","a103","a104","a105","a106","a107","a108","a109","a110","a111","a112","a113","a114","a115","a116","a117","a118","a119","a120","a121","a122","a123","a124","a125","a126","a127","a128","a129","a130","a131","a132","a133","a134","a135","a136","a137","a138","a139","a140","a141","a142","a143","a144","a145","a146","a147","a148","a149","a150","a151","a152","a153","a154","a155","a156","a157","a158","a159","a160","a161","a162","a163","a164","a165","a166","a167","a168","a169","a170","a171","a172","a173","a174","a175","a176","a177","a178","a179","a180","a181","a182","a183","a184","a185","a186","a187","a188","a189","a190","a191","a192","a193","a194","a195","a196","a197","a198","a199","a200","a201","a202","a203","a204","a205","a206","a207","a208","a209","a210","a211","a212","a213","a214","a215","a216","a217","a218","a219","a220","a221","a222","a223","a224","a225","a226","a227","a228","a229","a230","a231","a232","a233","a234","a235","a236","a237","a238","a239","a240","a241","a242","a243","a244","a245","a246","a247","a248","a249","a250","a251","a252","a253","a254","a255","s20","s21","s22","s23","s24","s25","s26","s27","s28","s29","s30","s31","s36","s37","s38","s39","s40","s41","s42","s43","s44","s45","s46","s47","s48","s49","s50","s51","s52","s53","s54","s55","s56","s57","s58","s59","s60","s61","s62","s63","s64","s65","s66","s67","s68","s69","s70","s71","s72","s73","s74","s75","s76","s77","s78","s79","s80","s81","s82","s83","s84","s85","s86","s87","s88","s89","s90","s91","s92","s93","s94","s95","s96","s97","s98","s99");
  if (__builtin_expect(flag != 0, 0)) {
    asm volatile("s_waitcnt vmcnt(0)" : "+s"(ka) :: "memory");
    const unsigned head = (bid >> 4) * 8 + (bid & 7), w = tid >> 6;
    slow_rows((const unsigned short*)ka[0] + (size_t)head * SQ * D, (const unsigned short*)ka[1] + (size_t)head * SK * D,
              (const unsigned short*)ka[2] + (size_t)head * SK * D, (unsigned short*)ka[3] + (size_t)head * SQ * D,
              (int)(((bid >> 3) & 1) * 256 + w * 64), (float*)(lds + 32768 + w * 16384));
  }
}

extern "C" void kernel_launch(void* const* d_in, const int* in_sizes, int n_in, void* d_out, int out_size, void* d_ws, size_t ws_size,
                              hipStream_t stream) {
  mha_fwd_d256<<<256, 256, 0, stream>>>((const unsigned short*)d_in[0], (const unsigned short*)d_in[1], (const unsigned short*)d_in[2],
                                        (unsigned short*)d_out
  );
}
```
